```python
import torch

def _softmax(x, axis=-1):
    return torch.softmax(x, dim=axis)
def _gelu(x):
    return torch.nn.functional.gelu(x, approximate="tanh")
def _silu(x):
    return torch.nn.functional.silu(x)
def _sigmoid(x):
    return torch.sigmoid(x)
def _layernorm(x, axis=-1, eps=1e-5):
    m = x.mean(dim=axis, keepdim=True)
    v = x.var(dim=axis, keepdim=True, unbiased=False)
    return (x - m) / torch.sqrt(v + eps)
def _scatter_add(tgt, idx, src, axis=0):
    return tgt.clone().index_add_(axis, idx, src)
def _segment_sum(src, seg, n):
    out = torch.zeros((n,) + src.shape[1:], dtype=src.dtype)
    return out.index_add_(0, seg, src)
def _pad(x, pw):
    flat = tuple(v for pair in reversed(pw) for v in pair)
    return torch.nn.functional.pad(x, flat)

kernel_name = 'concat_heads__ssm_conv_s4__grouped_kv_mqa__prompt'

def setup_inputs():

    _w = (lambda a: (lambda t: t.to(torch.bfloat16) if t.is_floating_point() else t)(torch.as_tensor(a)))
    def _A_setup():
        B, L, D, N = 1, 2048, 576, 128
        A_re = -(lambda _x: torch.exp(torch.as_tensor(_x)))((lambda *s: torch.randn(*s, dtype=torch.bfloat16) * 3.2)(D, N))
        A_im = torch.pi * (lambda *s: torch.randn(*s, dtype=torch.bfloat16) * 0.05)(D, N)
        B_re = (lambda *s: torch.randn(*s, dtype=torch.bfloat16) * 0.1)(D, N)
        B_im = (lambda *s: torch.randn(*s, dtype=torch.bfloat16) * 0.1)(D, N)
        C_re = (lambda *s: torch.randn(*s, dtype=torch.bfloat16) * 0.1)(D, N)
        C_im = (lambda *s: torch.randn(*s, dtype=torch.bfloat16) * 0.1)(D, N)
        log_dt = (lambda *s: torch.randn(*s, dtype=torch.bfloat16) * 0.05)(D) * 0.5 - 4.0
        Dskip = (lambda *s: torch.randn(*s, dtype=torch.bfloat16) * 0.05)(D)
        return {
            "u": _w((lambda *s: torch.randn(*s, dtype=torch.bfloat16) * 0.05)(B, L, D)),
            "A_re": _w(A_re),
            "A_im": _w(A_im),
            "B_re": _w(B_re),
            "B_im": _w(B_im),
            "C_re": _w(C_re),
            "C_im": _w(C_im),
            "log_dt": _w(log_dt),
            "Dskip": _w(Dskip),
        }
    _A_in = _A_setup()

    def _B_setup():
        B, S, D, H = 1, 2048, 576, 32
        Dh = D // H
        return {
            "x": _w((lambda *s: torch.randn(*s, dtype=torch.bfloat16) * 0.05)(B, S, D)),
            "wq": _w((lambda *s: torch.randn(*s, dtype=torch.bfloat16) * 0.8)(D, H * Dh)),
            "wk": _w((lambda *s: torch.randn(*s, dtype=torch.bfloat16) * 0.8)(D, Dh)),
            "wv": _w((lambda *s: torch.randn(*s, dtype=torch.bfloat16) * 0.05)(D, Dh)),
            "wo": _w((lambda *s: torch.randn(*s, dtype=torch.bfloat16) * 0.05)(H * Dh, D)),
        }
    _B_in = _B_setup()

    return {
        "A_u": _A_in["u"],
        "A_A_re": _A_in["A_re"],
        "A_A_im": _A_in["A_im"],
        "A_B_re": _A_in["B_re"],
        "A_B_im": _A_in["B_im"],
        "A_C_re": _A_in["C_re"],
        "A_C_im": _A_in["C_im"],
        "A_log_dt": _A_in["log_dt"],
        "A_Dskip": _A_in["Dskip"],
        "B_x": _B_in["x"],
        "B_wq": _B_in["wq"],
        "B_wk": _B_in["wk"],
        "B_wv": _B_in["wv"],
        "B_wo": _B_in["wo"],
    }

def reference(A_u, A_A_re, A_A_im, A_B_re, A_B_im, A_C_re, A_C_im, A_log_dt, A_Dskip, B_x, B_wq, B_wk, B_wv, B_wo):
    def _A_ref(u, A_re, A_im, B_re, B_im, C_re, C_im, log_dt, Dskip):
        Bsz, L, D = u.shape
        dt = torch.exp(log_dt)[:, None]
        A = A_re + 1j * A_im
        Bc = B_re + 1j * B_im
        Cc = C_re + 1j * C_im
        logdA = dt * A
        dB = dt * Bc
        l = (lambda x, dt: x.to(dt))(torch.arange(L), A_re.dtype)
        P = torch.exp(logdA[:, :, None] * l[None, None, :])
        K = torch.einsum("dn,dnl,dn->dl", Cc, P, dB).real
        L2 = 2 * L
        Kp = _pad(K, ((0, 0), (0, L)))
        up = (lambda x, *d: x.permute(*d))(u, 0, 2, 1)
        up = _pad(up, ((0, 0), (0, 0), (0, L)))
        Kf = (lambda x, axes=(-2,-1): torch.fft.rfft2(x.float(), dim=axes))(Kp[:, None, :], axes=(-2, -1))
        Uf = (lambda x, axes=(-2,-1): torch.fft.rfft2(x.float(), dim=axes))(up[:, :, None, :], axes=(-2, -1))
        Yf = Uf * Kf[None, :, :, :]
        y = (lambda x, s, axes=(-2,-1): torch.fft.irfft2(x, s=s, dim=axes).to(torch.bfloat16))(Yf, (1, L2), axes=(-2, -1))[:, :, 0, :L]
        y = (lambda x, *d: x.permute(*d))(y, 0, 2, 1) + u * Dskip[None, None, :]
        return y

    def _B_ref(x, wq, wk, wv, wo):
        B, S, D = x.shape
        H = 32
        Dh = D // H
        q = (lambda x, *d: x.permute(*d))((x @ wq).reshape(B, S, H, Dh), 0, 2, 1, 3)
        k = x @ wk
        v = x @ wv
        scores = torch.einsum("bhqd,bkd->bhqk", q, k) * (Dh ** -0.5)
        p = _softmax(scores, axis=-1)
        o = torch.einsum("bhqk,bkd->bhqd", p, v)
        return (lambda x, *d: x.permute(*d))(o, 0, 2, 1, 3).reshape(B, S, H * Dh) @ wo

    a = _A_ref(A_u, A_A_re, A_A_im, A_B_re, A_B_im, A_C_re, A_C_im, A_log_dt, A_Dskip)
    b = _B_ref(B_x, B_wq, B_wk, B_wv, B_wo)
    return torch.concatenate([a, b], axis=-1)
```

```cpp
#include <hip/hip_runtime.h>
#include <hip/hip_bf16.h>
typedef unsigned short u16;
__device__ __forceinline__ float bf2f(u16 v){ return __uint_as_float(((unsigned)v)<<16); }
__device__ __forceinline__ u16 f2bf(float f){ __bf16 b=(__bf16)f; return __builtin_bit_cast(u16,b); }
constexpr int L=2048, D=576, N=128, H=32, DH=18;

__global__ void k_gemm(const u16* A, const u16* B, u16* C, int M, int Nn, int K, int ldc) {
  int n = blockIdx.x*64 + (threadIdx.x&63); int m = blockIdx.y*4 + (threadIdx.x>>6);
  if (n>=Nn) return;
  float acc=0; for (int k=0;k<K;k++) acc += bf2f(A[m*K+k])*bf2f(B[k*Nn+n]);
  C[m*ldc+n]=f2bf(acc);
}
__global__ void k_kgen(const u16* Are,const u16* Aim,const u16* Bre,const u16* Bim,const u16* Cre,const u16* Cim,const u16* ldt,float* Kk){
  int d=blockIdx.y; int l=blockIdx.x*256+threadIdx.x;
  float dt=bf2f(f2bf(expf(bf2f(ldt[d]))));
  float lf=bf2f(f2bf((float)l));
  float acc=0;
  for(int n=0;n<N;n++){
    float ar=dt*bf2f(Are[d*N+n]), ai=dt*bf2f(Aim[d*N+n]);
    float br=dt*bf2f(Bre[d*N+n]), bi=dt*bf2f(Bim[d*N+n]);
    float cr=bf2f(Cre[d*N+n]), ci=bf2f(Cim[d*N+n]);
    float xr=cr*br-ci*bi, xi=cr*bi+ci*br;
    float e=expf(ar*lf); float s,c; sincosf(ai*lf,&s,&c);
    acc += e*(xr*c-xi*s);
  }
  Kk[d*L+l]=acc;
}
__global__ void k_conv(const u16* u,const float* Kk,const u16* Dskip,u16* out){
  int d=blockIdx.x*64+(threadIdx.x&63); int t=blockIdx.y*4+(threadIdx.x>>6);
  float acc=0; for(int s=0;s<=t;s++) acc+=Kk[d*L+s]*bf2f(u[(t-s)*D+d]);
  float y=bf2f(f2bf(acc)); float sk=bf2f(f2bf(bf2f(u[t*D+d])*bf2f(Dskip[d])));
  out[t*2*D+d]=f2bf(y+sk);
}
__global__ void k_attn(const u16* q,const u16* k,const u16* v,u16* o){
  int idx=blockIdx.x*256+threadIdx.x; int row=idx/H, h=idx%H;
  float qq[DH]; for(int i=0;i<DH;i++) qq[i]=bf2f(q[row*D+h*DH+i])*0.23570226f;
  float m=-1e30f,l=0,acc[DH]; for(int i=0;i<DH;i++) acc[i]=0;
  for(int j=0;j<L;j++){
    float s=0; for(int i=0;i<DH;i++) s+=qq[i]*bf2f(k[j*DH+i]);
    float mn=fmaxf(m,s); float a=__expf(m-mn), p=__expf(s-mn); l=l*a+p;
    for(int i=0;i<DH;i++) acc[i]=acc[i]*a+p*bf2f(v[j*DH+i]);
    m=mn;
  }
  for(int i=0;i<DH;i++) o[row*D+h*DH+i]=f2bf(acc[i]/l);
}
extern "C" void kernel_launch(void* const* d_in, const int* in_sizes, int n_in, void* d_out, int out_size, void* d_ws, size_t ws_size, hipStream_t stream) {
  const u16* u=(const u16*)d_in[0]; const u16* x=(const u16*)d_in[9];
  const u16 *wq=(const u16*)d_in[10], *wk=(const u16*)d_in[11], *wv=(const u16*)d_in[12], *wo=(const u16*)d_in[13];
  char* ws=(char*)d_ws; u16* q=(u16*)ws; u16* k=(u16*)(ws+(4<<20)); u16* v=(u16*)(ws+(5<<20)); u16* o=(u16*)(ws+(6<<20)); float* Kk=(float*)(ws+(10<<20));
  u16* out=(u16*)d_out;
  k_gemm<<<dim3(9,512),256,0,stream>>>(x,wq,q,L,D,D,D);
  k_gemm<<<dim3(1,512),256,0,stream>>>(x,wk,k,L,DH,D,DH);
  k_gemm<<<dim3(1,512),256,0,stream>>>(x,wv,v,L,DH,D,DH);
  k_kgen<<<dim3(8,D),256,0,stream>>>((const u16*)d_in[1],(const u16*)d_in[2],(const u16*)d_in[3],(const u16*)d_in[4],(const u16*)d_in[5],(const u16*)d_in[6],(const u16*)d_in[7],Kk);
  k_conv<<<dim3(9,512),256,0,stream>>>(u,Kk,(const u16*)d_in[8],out);
  k_attn<<<256,256,0,stream>>>(q,k,v,o);
  k_gemm<<<dim3(9,512),256,0,stream>>>(o,wo,out+D,L,D,D,2*D);
}
```

```cpp
#include <hip/hip_runtime.h>
#include <hip/hip_bf16.h>

typedef unsigned short u16;
typedef unsigned long long u64;
typedef __attribute__((ext_vector_type(8))) __bf16 bf16x8;
typedef __attribute__((ext_vector_type(2))) __bf16 bf16x2;
typedef __attribute__((ext_vector_type(8))) _Float16 f16x8;
typedef __attribute__((ext_vector_type(2))) _Float16 f16x2;
typedef __attribute__((ext_vector_type(4))) short s16x4;
typedef __attribute__((ext_vector_type(8))) short s16x8;
typedef __attribute__((ext_vector_type(4))) float f32x4;
typedef __attribute__((ext_vector_type(2))) float f32x2;
typedef __attribute__((ext_vector_type(16))) float f32x16;
typedef __attribute__((ext_vector_type(4))) unsigned u32x4;
typedef __attribute__((ext_vector_type(2))) unsigned u32x2;
#define LAS __attribute__((address_space(3)))
#define GAS __attribute__((address_space(1)))
typedef LAS char* lptr;
#define DI __device__ __forceinline__
#define SBAR() __builtin_amdgcn_sched_barrier(0)
#define PIN(x) asm volatile("" : "+v"(x))
#define RLX_AGENT __ATOMIC_RELAXED, __HIP_MEMORY_SCOPE_AGENT

constexpr int ROWB = 1152;
constexpr int KV_V = 1216, KV_REC = 2496, KV_CHUNK = 20480;
constexpr unsigned WS_EPOCH = 0, WS_KVF = 256, WS_OF = 4096, WS_KV = 16384, WS_O2 = WS_KV + 8 * KV_CHUNK, WS_END = WS_O2 + 2048 * ROWB;
constexpr int L_KV = 0, L_KC = 40960, KC_COPY = 4160, KC_CH = 4 * KC_COPY, L_KQ = 107520, KQ_CH = 1288, L_TAB = 112672, TAB_W = 11520;
constexpr int L_QX = L_TAB + 16, L_K0 = 124992, L_U = 128000, U_CH = 7680, L_WO = L_KC, L_OX = L_U, L_OT = L_U + 24576, L_PART = 0, L_MISC = 163328;
constexpr int QS = 17408, QNS = 6;
constexpr unsigned EP_STEP = 0x9E3779B9u;
constexpr float LOG2E = 1.4426950408889634f;
constexpr float QSCALE = 0.23570226039551584f * LOG2E;

#define STAMP_DECL
#define STAMP_AT(idx)
#define STAMP_FLUSH(kid)

DI unsigned pk2(float lo, float hi) { f32x2 v = {lo, hi}; bf16x2 b = __builtin_convertvector(v, bf16x2); return __builtin_bit_cast(unsigned, b); }
DI unsigned pkh(f32x2 v) { f16x2 b = __builtin_convertvector(v, f16x2); return __builtin_bit_cast(unsigned, b); }
DI float bflo(unsigned v) { return __uint_as_float(v << 16); }
DI float bfhi(unsigned v) { return __uint_as_float(v & 0xffff0000u); }
DI float sq2(unsigned v) { return bflo(v) * bflo(v) + bfhi(v) * bfhi(v); }

DI void glds16(const void* sbase, unsigned voff, unsigned lds_dst) {
  unsigned sv;
  asm volatile("s_mov_b32 %0, m0\n\ts_mov_b32 m0, %3\n\ts_nop 0\n\tglobal_load_lds_dwordx4 %1, %2\n\ts_mov_b32 m0, %0"
               : "=&s"(sv) : "v"(voff), "s"(sbase), "s"(lds_dst) : "memory");
}
DI void glds16_8lanes(const void* sbase, unsigned voff, unsigned lds_dst) {
  unsigned sv;
  asm volatile("s_mov_b32 %0, m0\n\ts_mov_b32 m0, %3\n\ts_mov_b64 exec, 0xff\n\tglobal_load_lds_dwordx4 %1, %2\n\ts_mov_b64 exec, -1\n\ts_mov_b32 m0, %0"
               : "=&s"(sv) : "v"(voff), "s"(sbase), "s"(lds_dst) : "memory");
}
template <int N> DI void wait_vm_bar() { asm volatile("s_waitcnt vmcnt(%0) lgkmcnt(0)\n\ts_barrier" ::"i"(N) : "memory"); }
DI void bar() { asm volatile("s_waitcnt lgkmcnt(0)\n\ts_barrier" ::: "memory"); }
DI void bar_raw() { asm volatile("s_barrier" ::: "memory"); }
DI bf16x8 lds_b128(lptr p) { return *(const LAS bf16x8*)p; }
DI u32x4 lds_u128(lptr p) { return *(const LAS u32x4*)p; }
DI s16x4 lds_tr(lptr p) { return __builtin_amdgcn_ds_read_tr16_b64_v4i16((LAS s16x4*)p); }
DI bf16x8 cat8(s16x4 a, s16x4 b) { s16x8 v = __builtin_shufflevector(a, b, 0, 1, 2, 3, 4, 5, 6, 7); return __builtin_bit_cast(bf16x8, v); }
#define MFMA16(a, b, c) __builtin_amdgcn_mfma_f32_16x16x32_bf16(a, b, c, 0, 0, 0)
#define MFMA32(a, b, c) __builtin_amdgcn_mfma_f32_32x32x16_bf16(a, b, c, 0, 0, 0)
#define MFMA32H(a, b, c) __builtin_amdgcn_mfma_f32_32x32x16_f16(a, b, c, 0, 0, 0)
#define MFMA32K8(a, b, c) __builtin_amdgcn_mfma_f32_32x32x8bf16_1k(a, b, c, 0, 0, 0)
#define EX(v) __builtin_amdgcn_exp2f(v)
#define PB(P) __builtin_bit_cast(bf16x8, P)

DI float wave_max(float v) {
#pragma unroll
  for (int o = 1; o < 64; o <<= 1) v = fmaxf(v, __shfl_xor(v, o));
  return v;
}
DI float other_half(float v) { auto rr = __builtin_amdgcn_permlane32_swap(__float_as_uint(v), __float_as_uint(v), false, false); return (threadIdx.x & 32) ? __uint_as_float(rr[0]) : __uint_as_float(rr[1]); }
DI bool spin_expired(u64 t0) { __builtin_amdgcn_s_sleep(1); return __builtin_amdgcn_s_memrealtime() - t0 > 400000ull; }
DI f32x2 cmul(f32x2 a, f32x2 b) { return f32x2{__builtin_fmaf(-a[1], b[1], a[0] * b[0]), __builtin_fmaf(a[1], b[0], a[0] * b[1])}; }
DI f32x2 cexp2(float lg, float rev) { rev -= __builtin_rintf(rev); const float e = EX(lg); return f32x2{e * __builtin_amdgcn_cosf(rev), e * __builtin_amdgcn_sinf(rev)}; }

__global__ __launch_bounds__(768) void mega_kernel(const char* __restrict__ u, const char* __restrict__ Are, const char* __restrict__ Aim, const char* __restrict__ Bre,
    const char* __restrict__ Bim, const char* __restrict__ Cre, const char* __restrict__ Cim, const char* __restrict__ ldt, const char* __restrict__ Dsk,
    const char* __restrict__ x, const char* __restrict__ wq, const char* __restrict__ wk, const char* __restrict__ wv, const char* __restrict__ wo,
    char* out, char* ws) {
  __shared__ __attribute__((aligned(16))) char smem[163840];
  const lptr lds = (lptr)smem;
  const unsigned lds0 = (unsigned)(size_t)lds;
  const int tid = threadIdx.x, lane = tid & 63, w = __builtin_amdgcn_readfirstlane(tid >> 6);
  const int b = blockIdx.x, xcd = b & 7, j = b >> 3, rb = xcd * 4 + (j & 3), hg = j >> 2;
  const int c = lane & 31, h = lane >> 5, r16 = lane & 15, g = lane >> 4, q4 = (lane & 15) >> 2, p4 = lane & 3;
  const __amdgpu_buffer_rsrc_t wsr = __builtin_amdgcn_make_buffer_rsrc(ws, 0, WS_END, 0x00020000);
  const f32x16 zero = {};
  STAMP_DECL; STAMP_AT(0);
  asm volatile("" :: "s"(u), "s"(Are), "s"(Aim), "s"(Bre), "s"(Bim), "s"(Cre), "s"(Cim), "s"(ldt), "s"(Dsk), "s"(x), "s"(wq), "s"(wk), "s"(wv), "s"(wo), "s"(out), "s"(ws));

  if (w < 8) {
    const int hd = w & 3, kh = w >> 2;
    {
      const int rot = (hg + 2 * (j & 3)) % 9;
      const int xrow = 8 * w + (lane >> 3);
      const unsigned xo = xrow * ROWB + (((lane & 7) ^ ((xrow >> 1) & 7)) * 16);
      const int c0 = 64 * w + lane, c1 = 512 + 8 * w + (lane & 7);
      const unsigned wo0 = (c0 / 9) * ROWB + (c0 % 9) * 16, wo1 = (c1 / 9) * ROWB + (c1 % 9) * 16;
      const char* xb = x + (size_t)rb * 64 * ROWB;
      const char* wb = wq + hg * 144;
      auto issue = [&](int s, int slot) {
        int st = s + rot; if (st >= 9) st -= 9;
        const unsigned dst = lds0 + slot * QS;
        glds16(xb + st * 128, xo, dst + w * 1024);
        glds16(wb + (size_t)st * 64 * ROWB, wo0, dst + 8192 + w * 1024);
        glds16_8lanes(wb + (size_t)st * 64 * ROWB, wo1, dst + 16384 + w * 128);
      };
#pragma unroll
      for (int s = 0; s < QNS; ++s) issue(s, s);
      bar_raw();
      lptr xa[4];
#pragma unroll
      for (int ks = 0; ks < 4; ++ks) xa[ks] = lds + (32 * kh + c) * 128 + (((2 * ks + h) ^ ((c >> 1) & 7)) * 16);
      const int gi = 4 * (g & 1) + p4;
      const lptr wa = lds + 8192 + (8 * h + q4) * 144 + ((36 * hd) & ~7) + 8 * (gi < 4 ? gi : 4);
      f32x16 acc = zero;
#pragma unroll
      for (int s = 0; s < 9; ++s) {
        const int issued = (QNS + (s > 0 ? s - 1 : 0)) < 9 ? (QNS + (s > 0 ? s - 1 : 0)) : 9;
        switch (issued - 1 - s) {
          case 0: wait_vm_bar<0>(); break; case 1: wait_vm_bar<3>(); break; case 2: wait_vm_bar<6>(); break;
          case 3: wait_vm_bar<9>(); break; case 4: wait_vm_bar<12>(); break; default: wait_vm_bar<15>(); break;
        }
        if (s == 0) STAMP_AT(1);
        const int so = (s % QNS) * QS;
        bf16x8 xf[4], wf[4];
#pragma unroll
        for (int ks = 0; ks < 4; ++ks) { xf[ks] = lds_b128(xa[ks] + so); wf[ks] = cat8(lds_tr(wa + so + ks * 16 * 144), lds_tr(wa + so + ks * 16 * 144 + 4 * 144)); }
        if (s > 0 && QNS + s - 1 < 9) issue(QNS + s - 1, (s - 1) % QNS);
#pragma unroll
        for (int ks = 0; ks < 4; ++ks) acc = MFMA32(wf[ks], xf[ks], acc);
      }
      const lptr qw = lds + L_QX + (hd * 64 + 32 * kh + c) * 48 + 8 * h - 4 * (hd & 1);
#pragma unroll
      for (int i = 0; i < 3; ++i) {
        *(LAS unsigned*)(qw + 16 * i) = pk2(acc[4 * i] * QSCALE, acc[4 * i + 1] * QSCALE);
        *(LAS unsigned*)(qw + 16 * i + 4) = pk2(acc[4 * i + 2] * QSCALE, acc[4 * i + 3] * QSCALE);
      }
    }
    bar();
    STAMP_AT(2);
    const u32x4 qau = lds_u128(lds + L_QX + (hd * 64 + 32 * kh + c) * 48 + 16 * h);
    const u32x4 qbu = lds_u128(lds + L_QX + (hd * 64 + 32 * (kh ^ 1) + c) * 48 + 16 * h);
    const unsigned q2lo = *(const LAS unsigned*)(lds + L_QX + (hd * 64 + ((lane + 32 * kh) & 63)) * 48 + 32);
    float n2;
    {
      const float pa = sq2(qau[0]) + sq2(qau[1]) + sq2(qau[2]) + sq2(qau[3]), pb = sq2(qbu[0]) + sq2(qbu[1]) + sq2(qbu[2]) + sq2(qbu[3]);
      auto rr = __builtin_amdgcn_permlane32_swap(__float_as_uint(pa), __float_as_uint(pb), false, false);
      n2 = __uint_as_float(rr[0]) + __uint_as_float(rr[1]) + sq2(q2lo);
    }
    const bf16x8 q1a = PB(qau), q1b = PB(qbu);
    lptr vb = lds + L_KV + kh * KV_REC + h * 304 + (c < 18 ? c : 18) * 16;
    lptr kb = lds + L_KV + kh * KV_REC + KV_V + lane * 16;
    lptr k2b = lds + L_KV + kh * KV_REC + KV_V + 1024 + c * 8;
    bar();
    STAMP_AT(3);
    const float kmx = *(const LAS float*)(lds + L_MISC + 4);
    bf16x8 kf = lds_b128(lds + L_K0 + kh * 1280 + lane * 16);
    s16x4 kf2 = *(const LAS s16x4*)(lds + L_K0 + kh * 1280 + 1024 + c * 8);
    const float bound = __builtin_amdgcn_sqrtf(n2 * kmx) * 1.01f;
    const bool fast = __all(bound <= 40.f);
    f32x16 Oa = zero, Ob = zero;
    float ma = 0.f, mb = 0.f;
    if (fast) {
      const unsigned nm = pk2(-bound, 0.f);
      const u32x2 qa = h ? u32x2{0u, 0u} : u32x2{q2lo, nm}, qb = h ? u32x2{q2lo, nm} : u32x2{0u, 0u};
      const s16x4 q2a = __builtin_bit_cast(s16x4, qa), q2b = __builtin_bit_cast(s16x4, qb);
      f32x16 Aa, Ab, Ba, Bb;
      Aa = MFMA32K8(kf2, q2a, zero); Ab = MFMA32K8(kf2, q2b, zero);
      Aa = MFMA32(kf, q1a, Aa); Ab = MFMA32(kf, q1b, Ab);
      bf16x8 vf0 = lds_b128(vb), vf1 = lds_b128(vb + 608);
      kf = lds_b128(kb); kf2 = *(const LAS s16x4*)k2b;
      u32x4 p0, p1;
#define GRP(MF, RD, S, i, P, jj) do { float e0 = EX(S[i]), e1 = EX(S[i + 1]); SBAR(); MF; RD; SBAR(); float e2 = EX(S[i + 2]), e3 = EX(S[i + 3]); P[jj] = pk2(e0, e1); P[jj + 1] = pk2(e2, e3); PIN(P); SBAR(); } while (0)
#define HALF1(Ca, Cb, Na) do { \
      GRP(Na = MFMA32K8(kf2, q2a, zero), , Ca, 4, p0, 2); \
      GRP(Oa = MFMA32(vf0, PB(p0), Oa), , Ca, 8, p1, 0); \
      GRP(Na = MFMA32(kf, q1a, Na), , Ca, 12, p1, 2); \
      GRP(Oa = MFMA32(vf1, PB(p1), Oa), , Cb, 0, p0, 0); } while (0)
#define HALF2(Cb, Na, Nb, off) do { \
      GRP(Nb = MFMA32K8(kf2, q2b, zero), kf2 = *(const LAS s16x4*)(k2b + (off)), Cb, 4, p0, 2); \
      GRP(Ob = MFMA32(vf0, PB(p0), Ob), vf0 = lds_b128(vb + (off)), Cb, 8, p1, 0); \
      GRP(Nb = MFMA32(kf, q1b, Nb), kf = lds_b128(kb + (off)), Cb, 12, p1, 2); \
      GRP(Ob = MFMA32(vf1, PB(p1), Ob), vf1 = lds_b128(vb + (off) + 608), Na, 0, p0, 0); } while (0)
      p0[0] = pk2(EX(Aa[0]), EX(Aa[1])); p0[1] = pk2(EX(Aa[2]), EX(Aa[3])); p0[2] = 0; p0[3] = 0; p1 = p0;
      for (int p = 0; p < 8; ++p) {
        HALF1(Aa, Ab, Ba); __builtin_amdgcn_s_setprio(2); HALF2(Ab, Ba, Bb, 2 * KV_REC);
        HALF1(Ba, Bb, Aa); __builtin_amdgcn_s_setprio(1); HALF2(Bb, Aa, Ab, 4 * KV_REC);
        HALF1(Aa, Ab, Ba); __builtin_amdgcn_s_setprio(0); HALF2(Ab, Ba, Bb, 6 * KV_REC);
        HALF1(Ba, Bb, Aa);
        const int adv = (p & 1) ? -KV_CHUNK : KV_CHUNK;
        vb += adv; kb += adv; k2b += adv;
        if (p < 7) bar();
        __builtin_amdgcn_s_setprio(3);
        HALF2(Bb, Aa, Ab, 0);
      }
      __builtin_amdgcn_s_setprio(0);
    } else {
      const u32x2 qa = h ? u32x2{0u, 0u} : u32x2{q2lo, 0u}, qb = h ? u32x2{q2lo, 0u} : u32x2{0u, 0u};
      const s16x4 q2a = __builtin_bit_cast(s16x4, qa), q2b = __builtin_bit_cast(s16x4, qb);
      ma = -INFINITY; mb = -INFINITY;
      for (int i = 0; i < 32; ++i) {
        if (i && (i & 3) == 0) bar();
        const int off = ((i >> 2) & 1) * KV_CHUNK + (i & 3) * 2 * KV_REC;
        f32x16 Sa = MFMA32K8(kf2, q2a, zero), Sb = MFMA32K8(kf2, q2b, zero);
        Sa = MFMA32(kf, q1a, Sa); Sb = MFMA32(kf, q1b, Sb);
        kf = lds_b128(kb + off); kf2 = *(const LAS s16x4*)(k2b + off);
        const bf16x8 vf0 = lds_b128(vb + off), vf1 = lds_b128(vb + off + 608);
        float ta = ma, tb = mb;
#pragma unroll
        for (int r = 0; r < 16; ++r) { ta = fmaxf(ta, Sa[r]); tb = fmaxf(tb, Sb[r]); }
        ta = fmaxf(ta, other_half(ta)); tb = fmaxf(tb, other_half(tb));
        const float fa = EX(ma - ta), fb = EX(mb - tb);
        ma = ta; mb = tb;
        u32x4 pp[4];
#pragma unroll
        for (int r = 0; r < 8; ++r) {
          Oa[2 * r] *= fa; Oa[2 * r + 1] *= fa; Ob[2 * r] *= fb; Ob[2 * r + 1] *= fb;
          pp[r >> 2][r & 3] = pk2(EX(Sa[2 * r] - ma), EX(Sa[2 * r + 1] - ma));
          pp[2 + (r >> 2)][r & 3] = pk2(EX(Sb[2 * r] - mb), EX(Sb[2 * r + 1] - mb));
        }
        Oa = MFMA32(vf0, PB(pp[0]), Oa); Ob = MFMA32(vf0, PB(pp[2]), Ob);
        Oa = MFMA32(vf1, PB(pp[1]), Oa); Ob = MFMA32(vf1, PB(pp[3]), Ob);
      }
    }
    STAMP_AT(4);
    {
      const lptr ox = lds + L_OX + w * 3072 + lane * 16;
      *(LAS f32x4*)(ox) = f32x4{Ob[0], Ob[1], Ob[2], Ob[3]};
      *(LAS f32x4*)(ox + 1024) = f32x4{Ob[4], Ob[5], Ob[6], Ob[7]};
      *(LAS f32x4*)(ox + 2048) = f32x4{Ob[8], Ob[9], Ob[10], mb};
    }
    bar();
    {
      const lptr ox = lds + L_OX + (w ^ 4) * 3072 + lane * 16;
      const f32x4 t0 = *(const LAS f32x4*)(ox), t1 = *(const LAS f32x4*)(ox + 1024), t2 = *(const LAS f32x4*)(ox + 2048);
      const float mm = fmaxf(ma, t2[3]);
      float fa = EX(ma - mm), fo = EX(t2[3] - mm);
      const float inv = 1.f / (Oa[10] * fa + t2[2] * fo);
      fa *= inv; fo *= inv;
      const lptr ot = lds + L_OT + (32 * kh + c) * 144 + 36 * hd + 8 * h;
      *(LAS unsigned*)(ot) = pk2(Oa[0] * fa + t0[0] * fo, Oa[1] * fa + t0[1] * fo);
      *(LAS unsigned*)(ot + 4) = pk2(Oa[2] * fa + t0[2] * fo, Oa[3] * fa + t0[3] * fo);
      *(LAS unsigned*)(ot + 16) = pk2(Oa[4] * fa + t1[0] * fo, Oa[5] * fa + t1[1] * fo);
      *(LAS unsigned*)(ot + 20) = pk2(Oa[6] * fa + t1[2] * fo, Oa[7] * fa + t1[3] * fo);
      if (h == 0) *(LAS unsigned*)(ot + 32) = pk2(Oa[8] * fa + t2[0] * fo, Oa[9] * fa + t2[1] * fo);
    }
  } else {
    const int hw = w - 8, hl = tid - 512;
    const bool ssm = j < 18, kvp = j >= 24;
    const unsigned ep = __hip_atomic_load((GAS unsigned*)(ws + WS_EPOCH), RLX_AGENT);
    const int ch0 = xcd * 72 + j * 4;
    u32x2 ur[8];
    unsigned dsk = 0;
    if (ssm) {
      const int d = ch0 + hw;
      const unsigned po = d * 256 + lane * 4;
      const unsigned vdt = *(const u16*)(ldt + d * 2);
      const unsigned vAr = *(const unsigned*)(Are + po), vAi = *(const unsigned*)(Aim + po), vBr = *(const unsigned*)(Bre + po), vBi = *(const unsigned*)(Bim + po);
      const unsigned vCr = *(const unsigned*)(Cre + po), vCi = *(const unsigned*)(Cim + po);
      SBAR();
      dsk = *(const u16*)(Dsk + d * 2);
#pragma unroll
      for (int e = 0; e < 8; ++e) ur[e] = *(const u32x2*)(u + (size_t)(8 * hl + e) * ROWB + ch0 * 2);
      SBAR(); bar_raw(); SBAR();
      const float dt = bflo(pk2(expf(bflo(vdt)), 0.f));
      float a2[2], rv[2]; f32x2 cc[2];
#pragma unroll
      for (int bt = 0; bt < 2; ++bt) {
        const float ar = bt ? bfhi(vAr) : bflo(vAr), ai = bt ? bfhi(vAi) : bflo(vAi), br = dt * (bt ? bfhi(vBr) : bflo(vBr)), bi = dt * (bt ? bfhi(vBi) : bflo(vBi));
        const float cr = bt ? bfhi(vCr) : bflo(vCr), ci = bt ? bfhi(vCi) : bflo(vCi);
        a2[bt] = dt * ar * LOG2E; rv[bt] = dt * ai * 0.15915494309189535f;
        cc[bt] = f32x2{cr * br - ci * bi, -(cr * bi + ci * br)};
      }
      const float amax = wave_max(fmaxf(a2[0], a2[1]));
      const float cmax = wave_max(fmaxf(fmaxf(fabsf(cc[0][0]), fabsf(cc[0][1])), fmaxf(fabsf(cc[1][0]), fabsf(cc[1][1]))));
      const int ce = __builtin_amdgcn_frexp_expf(cmax);
      const lptr tw = lds + L_TAB + hw * TAB_W + lane * 4;
      const lptr tra = lds + L_TAB + hw * TAB_W + (c < 20 ? c : 20) * 272 + 16 * h, trb = lds + L_TAB + hw * TAB_W + c * 272 + 16 * h;
      f32x16 kacc = zero;
#define TW(row, v) (*(LAS unsigned*)(tw + (row) * 272) = pkh(v))
#pragma unroll
      for (int bt = 0; bt < 2; ++bt) {
        const float an = a2[bt] - amax;
        const f32x2 E1 = cexp2(an, -rv[bt]), E256 = cexp2(256.f * an, -256.f * rv[bt]), E8 = cexp2(8.f * an, 8.f * rv[bt]);
        const f32x2 x0 = {__builtin_amdgcn_ldexpf(cc[bt][0], 14 - ce), __builtin_amdgcn_ldexpf(cc[bt][1], 14 - ce)};
        bar();
        const f32x2 E2 = cmul(E1, E1), E4 = cmul(E2, E2);
        {
          const f32x2 x1 = cmul(x0, E1), x2 = cmul(x0, E2), x3 = cmul(x1, E2);
          TW(0, x0); TW(1, x1); TW(2, x2); TW(3, x3);
          TW(4, cmul(x0, E4)); TW(5, cmul(x1, E4)); TW(6, cmul(x2, E4)); TW(7, cmul(x3, E4));
          const f32x2 y0 = cmul(x0, E256), y1 = cmul(y0, E2);
          TW(8, y0); TW(9, y1); TW(10, cmul(y0, E4)); TW(11, cmul(y1, E4));
          f32x2 z = cmul(y0, E256);
          TW(12, z); TW(13, cmul(z, E4)); z = cmul(z, E256); TW(14, z); TW(15, cmul(z, E4));
#pragma unroll
          for (int i = 16; i < 21; ++i) { z = cmul(z, E256); TW(i, z); }
        }
        f16x8 af[8];
#pragma unroll
        for (int ks = 0; ks < 8; ++ks) af[ks] = *(const LAS f16x8*)(tra + 32 * ks);
        bar();
        const f32x2 E16 = cmul(E8, E8), E32 = cmul(E16, E16);
        f32x2 y[4]; y[0] = f32x2{16384.f, 0.f}; y[1] = E8 * 16384.f; y[2] = E16 * 16384.f; y[3] = cmul(y[1], E16);
#pragma unroll
        for (int i = 0; i < 8; ++i) {
#pragma unroll
          for (int k = 0; k < 4; ++k) { TW(4 * i + k, y[k]); if (i < 7) y[k] = cmul(y[k], E32); }
          if (i == 3) bar();
        }
        f16x8 bfr[8];
#pragma unroll
        for (int ks = 0; ks < 8; ++ks) bfr[ks] = *(const LAS f16x8*)(trb + 32 * ks);
        bar();
#pragma unroll
        for (int ks = 0; ks < 8; ++ks) kacc = MFMA32H(af[ks], bfr[ks], kacc);
      }
      bar();
      {
        const float ec = (float)(ce - 28), c8 = (float)(8 * c);
        float kv_[12];
#pragma unroll
        for (int r = 0; r < 12; ++r) {
          const int b0 = r < 4 ? r : r < 8 ? 256 + 2 * (r - 4) : 1024 + 256 * (r - 8);
          const int b1 = r < 4 ? 4 + r : r < 8 ? 512 + 256 * ((r - 4) >> 1) + 4 * ((r - 4) & 1) : 2048;
          const float l = c8 + (h ? (float)b1 : (float)b0);
          kv_[r] = kacc[r] * EX(__builtin_fmaf(amax, l, ec));
        }
        const lptr kq = lds + L_KQ + hw * KQ_CH;
        *(LAS u32x2*)(kq + (8 * c + 4 * h) * 2) = u32x2{pk2(kv_[0], kv_[1]), pk2(kv_[2], kv_[3])};
        *(LAS unsigned*)(kq + (h ? 384 + 2 * c : 256 + 4 * c) * 2) = pk2(kv_[4], kv_[5]);
        *(LAS unsigned*)(kq + (h ? 448 + 2 * c : 258 + 4 * c) * 2) = pk2(kv_[6], kv_[7]);
        const unsigned k89 = pk2(kv_[8], kv_[9]), kab = pk2(kv_[10], kv_[11]);
        if (h == 0) {
          *(LAS u16*)(kq + (512 + c) * 2) = (u16)k89; *(LAS u16*)(kq + (544 + c) * 2) = (u16)(k89 >> 16);
          *(LAS u16*)(kq + (576 + c) * 2) = (u16)kab; *(LAS u16*)(kq + (608 + c) * 2) = (u16)(kab >> 16);
        } else if (c == 0) *(LAS unsigned*)(kq + 640 * 2) = k89 & 0xffffu;
      }
    } else if (kvp) {
      const int t = xcd * 8 + (j - 24), tq = hw & 1, sel = hw >> 1;
      const char* wsrc = sel ? wv : wk;
      u32x4 wa[5][2]; unsigned wb[5];
#pragma unroll
      for (int i = 0; i < 5; ++i) {
        int k = tq * 64 + lane + 128 * i; if (k > 575) k = 575;
        const char* src = wsrc + k * 36;
        __builtin_memcpy(&wa[i][0], __builtin_assume_aligned(src, 4), 16);
        __builtin_memcpy(&wa[i][1], __builtin_assume_aligned(src + 16, 4), 16);
        wb[i] = *(const unsigned*)(src + 32);
      }
      bf16x8 xf[18];
      const char* xr = x + (size_t)(t * 32 + 16 * tq + r16) * ROWB + g * 16;
#pragma unroll
      for (int ks = 0; ks < 18; ++ks) xf[ks] = *(const bf16x8*)(xr + ks * 64);
      const lptr img = lds + L_TAB + sel * 23040;
#pragma unroll
      for (int i = 0; i < 5; ++i) {
        int k = tq * 64 + lane + 128 * i; if (k > 575) k = 575;
        const lptr dst = img + k * 40;
        *(LAS u32x2*)(dst) = u32x2{wa[i][0][0], wa[i][0][1]}; *(LAS u32x2*)(dst + 8) = u32x2{wa[i][0][2], wa[i][0][3]};
        *(LAS u32x2*)(dst + 16) = u32x2{wa[i][1][0], wa[i][1][1]}; *(LAS u32x2*)(dst + 24) = u32x2{wa[i][1][2], wa[i][1][3]};
        *(LAS u32x2*)(dst + 32) = u32x2{wb[i], 0u};
      }
      bar();
      const lptr ma_ = img + (8 * g + q4) * 40 + 8 * p4, ra_ = img + (8 * g + q4) * 40 + 32;
      f32x4 aM = {0.f, 0.f, 0.f, 0.f}, aR = aM;
      float n2 = 0.f;
      const int kh_ = t & 1, ti = t >> 1;
      if (sel == 0) {
#pragma unroll
        for (int ks = 0; ks < 18; ++ks) {
          aM = MFMA16(cat8(lds_tr(ma_ + ks * 1280), lds_tr(ma_ + ks * 1280 + 160)), xf[ks], aM);
          aR = MFMA16(cat8(lds_tr(ra_ + ks * 1280), lds_tr(ra_ + ks * 1280 + 160)), xf[ks], aR);
        }
        const int rk = (ti + 31) & 31;
        char* kd = ws + WS_KV + (rk >> 2) * KV_CHUNK + ((rk & 3) * 2 + kh_) * KV_REC + KV_V;
        const unsigned k01 = pk2(aM[0], aM[1]), k23 = pk2(aM[2], aM[3]), kr = pk2(aR[0], aR[1]);
        __hip_atomic_store((GAS u64*)(kd + (g >> 1) * 512 + (16 * tq + r16) * 16 + (g & 1) * 8), (u64)k01 | ((u64)k23 << 32), RLX_AGENT);
        if (g == 0) __hip_atomic_store((GAS u64*)(kd + 1024 + (16 * tq + r16) * 8), (u64)kr | (0x3f80ull << 32), RLX_AGENT);
        n2 = sq2(k01) + sq2(k23) + (g == 0 ? sq2(kr) : 0.f);
        n2 += __shfl_xor(n2, 16); n2 += __shfl_xor(n2, 32);
        n2 = wave_max(n2);
      } else {
#pragma unroll
        for (int ks = 0; ks < 18; ++ks) {
          aM = MFMA16(xf[ks], cat8(lds_tr(ma_ + ks * 1280), lds_tr(ma_ + ks * 1280 + 160)), aM);
          aR = MFMA16(xf[ks], cat8(lds_tr(ra_ + ks * 1280), lds_tr(ra_ + ks * 1280 + 160)), aR);
        }
        char* vd = ws + WS_KV + (ti >> 2) * KV_CHUNK + ((ti & 3) * 2 + kh_) * KV_REC + tq * 608 + (g & 1) * 304 + (g >> 1) * 8;
        __hip_atomic_store((GAS u64*)(vd + r16 * 16), (u64)pk2(aM[0], aM[1]) | ((u64)pk2(aM[2], aM[3]) << 32), RLX_AGENT);
        if (r16 < 3) __hip_atomic_store((GAS u64*)(vd + (16 + r16) * 16), r16 == 2 ? 0x3f803f803f803f80ull : ((u64)pk2(aR[0], aR[1]) | ((u64)pk2(aR[2], aR[3]) << 32)), RLX_AGENT);
      }
      unsigned tg;
      asm volatile("s_waitcnt vmcnt(0)\n\tv_add_u32 %0, %2, %1" : "=v"(tg) : "v"(ep), "s"(EP_STEP) : "memory");
      if (lane == 0) __hip_atomic_store((GAS u64*)(ws + WS_KVF) + t * 4 + hw, ((u64)tg << 32) | __float_as_uint(n2), RLX_AGENT);
      STAMP_AT(1);
#pragma unroll 1
      for (int i = 0; i < 9; ++i) bar_raw();
    } else {
#pragma unroll 1
      for (int i = 0; i < 10; ++i) bar_raw();
    }
    bar();
    STAMP_AT(2);
    if (ssm) {
#pragma unroll
      for (int cc = 0; cc < 4; ++cc) {
        u32x4 v;
#pragma unroll
        for (int e = 0; e < 4; ++e) v[e] = __builtin_amdgcn_perm(ur[2 * e + 1][cc >> 1], ur[2 * e][cc >> 1], (cc & 1) ? 0x07060302u : 0x05040100u);
        *(LAS u32x4*)(lds + L_U + cc * U_CH + (32 + (hl >> 2)) * 80 + (hl & 3) * 16) = v;
      }
#pragma unroll
      for (int i = 0; i < 3; ++i) if (lane + 64 * i < 160) *(LAS u32x4*)(lds + L_U + hw * U_CH + (lane + 64 * i) * 16) = u32x4{0u, 0u, 0u, 0u};
    }
    {
      float km = 0.f;
      unsigned tag;
      asm volatile("v_add_u32 %0, %2, %1" : "=v"(tag) : "v"(ep), "s"(EP_STEP));
      const u64 t0 = __builtin_amdgcn_s_memrealtime();
      for (;;) {
        bool ok = true; km = 0.f;
#pragma unroll
        for (int k = 0; k < 4; ++k) { const u64 v = __hip_atomic_load((GAS u64*)(ws + WS_KVF) + lane + 64 * k, RLX_AGENT); ok &= (unsigned)(v >> 32) == tag; km = fmaxf(km, __uint_as_float((unsigned)v)); }
        if (__all(ok) || spin_expired(t0)) break;
      }
      __builtin_amdgcn_fence(__ATOMIC_ACQUIRE, "wavefront");
      if (hw == 0) { km = wave_max(km); if (lane == 0) *(LAS u32x2*)(lds + L_MISC) = u32x2{tag, __float_as_uint(km)}; }
    }
    STAMP_AT(3);
    u32x4 cv[5];
    {
      u32x4 c0[5];
#pragma unroll
      for (int i = 0; i < 5; ++i) c0[i] = __builtin_amdgcn_raw_buffer_load_b128(wsr, WS_KV + hl * 16 + i * 4096, 0, 16);
      const int kc = hl < 160 ? hl : 159;
      const u32x4 k0 = __builtin_amdgcn_raw_buffer_load_b128(wsr, WS_KV + 7 * KV_CHUNK + (6 + (kc >= 80)) * KV_REC + KV_V + (kc >= 80 ? kc - 80 : kc) * 16, 0, 16);
#pragma unroll
      for (int i = 0; i < 5; ++i) cv[i] = __builtin_amdgcn_raw_buffer_load_b128(wsr, WS_KV + KV_CHUNK + hl * 16 + i * 4096, 0, 16);
      if (ssm) {
        const LAS u16* kq = (const LAS u16*)(lds + L_KQ + hw * KQ_CH);
        const lptr kc_ = lds + L_KC + hw * KC_CH;
#pragma unroll
        for (int it = 0; it < 4; ++it) {
          const int s0 = 2047 - 8 * (lane + 64 * it);
          unsigned dd[6];
#pragma unroll
          for (int e = 0; e < 6; ++e) {
            const int sa = s0 - 2 * e, sb = sa - 1;
            const unsigned bits = pk2((float)sa, (float)sb);
            int ia = (int)(bits & 0xffffu) - 0x4280, ib = (int)(bits >> 16) - 0x4280;
            if (it == 3) { ia = sa < 0 ? 641 : sa < 256 ? sa : ia; ib = sb < 0 ? 641 : sb < 256 ? sb : ib; }
            dd[e] = (unsigned)kq[ia] | ((unsigned)kq[ib] << 16);
          }
          unsigned al[5];
#pragma unroll
          for (int e = 0; e < 5; ++e) al[e] = __builtin_amdgcn_alignbit(dd[e + 1], dd[e], 16);
          const lptr dst = kc_ + (lane + 64 * it) * 16;
          *(LAS u32x4*)(dst) = u32x4{dd[0], dd[1], dd[2], dd[3]};
          *(LAS u32x4*)(dst + KC_COPY) = u32x4{al[0], al[1], al[2], al[3]};
          *(LAS u32x4*)(dst + 2 * KC_COPY) = u32x4{dd[1], dd[2], dd[3], dd[4]};
          *(LAS u32x4*)(dst + 3 * KC_COPY) = u32x4{al[1], al[2], al[3], al[4]};
        }
        if (lane < 16) *(LAS u32x4*)(kc_ + (lane >> 2) * KC_COPY + 4096 + (lane & 3) * 16) = u32x4{0u, 0u, 0u, 0u};
      }
#pragma unroll
      for (int i = 0; i < 5; ++i) *(LAS u32x4*)(lds + L_KV + hl * 16 + i * 4096) = c0[i];
      *(LAS u32x4*)(lds + L_K0 + kc * 16) = k0;
    }
    bar();
    STAMP_AT(4);
    f32x16 y0 = zero, y1 = zero;
    lptr ka = lds + L_KC + hw * KC_CH + (3 - (c & 3)) * KC_COPY + (28 - (c & 28) + 8 * h) * 2 + 24 * 64;
    lptr ua = lds + L_U + hw * U_CH + (c + 1) * 80 + 16 * h + 24 * 80;
    unsigned woff[9];
#pragma unroll
    for (int i = 0; i < 9; ++i) { const int cc = 64 * i + lane; woff[i] = (cc / 9) * ROWB + (cc % 9) * 16; }
#pragma unroll 1
    for (int p = 0; p < 7; ++p) {
#pragma unroll
      for (int i = 0; i < 5; ++i) *(LAS u32x4*)(lds + L_KV + ((p + 1) & 1) * KV_CHUNK + hl * 16 + i * 4096) = cv[i];
      if (p < 6) {
#pragma unroll
        for (int i = 0; i < 5; ++i) cv[i] = __builtin_amdgcn_raw_buffer_load_b128(wsr, WS_KV + hl * 16 + i * 4096, (p + 2) * KV_CHUNK, 16);
      }
      if (p >= 4) {
        const int st = hw + 4 * (p - 4);
        if (st < 9) {
#pragma unroll
          for (int i = 0; i < 9; ++i) glds16(wo + hg * 144 + (size_t)st * 64 * ROWB, woff[i], lds0 + L_WO + st * 9216 + i * 1024);
        }
      }
      if (ssm) {
        if (p < 4) {
          if (p == 0) {
#pragma unroll
            for (int ks = 0; ks < 2; ++ks) {
              const int rel = c - 16 * ks - 8 * h;
              u32x4 df;
#pragma unroll
              for (int e = 0; e < 4; ++e) df[e] = (rel >> 1) == e ? dsk << (16 * (rel & 1)) : 0u;
              y0 = MFMA32(PB(df), lds_b128(ua + 7 * 80 + 32 * ks), y0);
              y1 = MFMA32(PB(df), lds_b128(ua + 39 * 80 + 32 * ks), y1);
            }
          }
          {
            bf16x8 an[2], af[2], b0[2], b1[2];
#define CLD(bf_, st_) do { const int ko = (7 - ((st_) >> 1)) * 64 + 32 * ((st_) & 1), uo = (7 - ((st_) >> 1)) * 80 + 32 * ((st_) & 1); \
              an[bf_] = cat8(*(const volatile LAS s16x4*)(ka + ko + 2048), *(const volatile LAS s16x4*)(ka + ko + 2048 + 8)); b0[bf_] = lds_b128(ua + uo); \
              b1[bf_] = lds_b128(ua + uo + 32 * 80); af[bf_] = cat8(*(const volatile LAS s16x4*)(ka + ko), *(const volatile LAS s16x4*)(ka + ko + 8)); } while (0)
            CLD(0, 0);
#pragma unroll
            for (int st = 0; st < 16; ++st) {
              SBAR();
              if (st < 15) CLD((st + 1) & 1, st + 1);
              SBAR();
              y0 = MFMA32(an[st & 1], b0[st & 1], y0); y1 = MFMA32(an[st & 1], b1[st & 1], y1); y1 = MFMA32(af[st & 1], b0[st & 1], y1);
            }
          }
          ka -= 8 * 64; ua -= 8 * 80;
        } else if (p == 4) {
          const lptr yw = lds + L_U + hw * U_CH + (32 + c) * 80 + 8 * h;
#pragma unroll
          for (int i = 0; i < 4; ++i) {
            *(LAS u32x2*)(yw + 16 * i) = u32x2{pk2(y0[4 * i], y0[4 * i + 1]), pk2(y0[4 * i + 2], y0[4 * i + 3])};
            *(LAS u32x2*)(yw + 32 * 80 + 16 * i) = u32x2{pk2(y1[4 * i], y1[4 * i + 1]), pk2(y1[4 * i + 2], y1[4 * i + 3])};
          }
        } else if (p == 5) {
          u32x4 yc[4];
#pragma unroll
          for (int cc = 0; cc < 4; ++cc) yc[cc] = lds_u128(lds + L_U + cc * U_CH + (32 + (hl >> 2)) * 80 + (hl & 3) * 16);
#pragma unroll
          for (int e = 0; e < 8; ++e) {
            const unsigned sl = (e & 1) ? 0x07060302u : 0x05040100u;
            *(u32x2*)(out + (size_t)(8 * hl + e) * (2 * ROWB) + ch0 * 2) = u32x2{__builtin_amdgcn_perm(yc[1][e >> 1], yc[0][e >> 1], sl), __builtin_amdgcn_perm(yc[3][e >> 1], yc[2][e >> 1], sl)};
          }
        }
      }
      bar();
    }
    STAMP_AT(5);
    wait_vm_bar<0>();
  }
  bar();
  if (tid < 576) {
    const int cell = tid >> 6, tok = lane, gc = 9 * hg + cell;
    const u32x4 v = lds_u128(lds + L_OT + tok * 144 + cell * 16);
    __builtin_amdgcn_raw_buffer_store_b128(v, wsr, WS_O2 + (((rb * 4 + (tok >> 4)) * 18 + (gc >> 2)) * 64 + (gc & 3) * 16 + (tok & 15)) * 16, 0, 16);
  }
  const unsigned tag = __builtin_amdgcn_readfirstlane(*(const LAS unsigned*)(lds + L_MISC));
  const int rt = w & 3, kt = w >> 2;
  bf16x8 wf[6][5];
  {
    const lptr wa = lds + L_WO + (192 * kt + 8 * g + q4) * 144 + 8 * p4;
#pragma unroll
    for (int ks = 0; ks < 6; ++ks)
#pragma unroll
      for (int t = 0; t < 5; ++t) wf[ks][t] = cat8(lds_tr(wa + ks * 32 * 144 + 32 * t), lds_tr(wa + ks * 32 * 144 + 4 * 144 + 32 * t));
  }
  wait_vm_bar<0>();
  if (tid == 0) __hip_atomic_store((GAS unsigned*)(ws + WS_OF) + rb * 32 + hg, tag, RLX_AGENT);
  STAMP_AT(5);
  {
    const u64 t0 = __builtin_amdgcn_s_memrealtime();
    for (;;) {
      const unsigned v = __hip_atomic_load((GAS unsigned*)(ws + WS_OF) + rb * 32 + (lane & 7), RLX_AGENT);
      if (__all(v == tag) || spin_expired(t0)) break;
    }
    __builtin_amdgcn_fence(__ATOMIC_ACQUIRE, "wavefront");
  }
  STAMP_AT(6);
  f32x4 acc[5] = {};
  {
    u32x4 of[6];
#pragma unroll
    for (int ks = 0; ks < 6; ++ks) of[ks] = __builtin_amdgcn_raw_buffer_load_b128(wsr, WS_O2 + (((rb * 4 + rt) * 18 + 6 * kt + ks) * 64 + lane) * 16, 0, 16);
#pragma unroll
    for (int ks = 0; ks < 6; ++ks)
#pragma unroll
      for (int t = 0; t < 5; ++t) acc[t] = MFMA16(wf[ks][t], PB(of[ks]), acc[t]);
  }
  if (kt > 0) {
#pragma unroll
    for (int t = 0; t < 5; ++t) *(LAS f32x4*)(lds + L_PART + (((kt - 1) * 4 + rt) * 5 + t) * 1024 + lane * 16) = acc[t];
  }
  bar();
  if (kt == 0) {
#pragma unroll
    for (int t = 0; t < 5; ++t) {
      const f32x4 s = acc[t] + *(const LAS f32x4*)(lds + L_PART + (rt * 5 + t) * 1024 + lane * 16) + *(const LAS f32x4*)(lds + L_PART + ((4 + rt) * 5 + t) * 1024 + lane * 16);
      if (t < 4 || g < 2) *(u32x2*)(out + (size_t)(rb * 64 + rt * 16 + r16) * (2 * ROWB) + ROWB + hg * 144 + t * 32 + g * 8) = u32x2{pk2(s[0], s[1]), pk2(s[2], s[3])};
    }
  } else if (b == 0 && w == 11) {
    const u64 t0 = __builtin_amdgcn_s_memrealtime();
    for (;;) {
      bool ok = true;
#pragma unroll
      for (int k = 0; k < 4; ++k) ok &= __hip_atomic_load((GAS unsigned*)(ws + WS_OF) + (lane >> 1) * 32 + (lane & 1) * 4 + k, RLX_AGENT) == tag;
      if (__all(ok) || spin_expired(t0)) break;
    }
    if (lane == 0) __hip_atomic_store((GAS unsigned*)(ws + WS_EPOCH), tag, RLX_AGENT);
  }
  STAMP_FLUSH(0);
}

extern "C" void kernel_launch(void* const* d_in, const int* in_sizes, int n_in, void* d_out, int out_size, void* d_ws, size_t ws_size, hipStream_t stream) {
  mega_kernel<<<256, 768, 0, stream>>>((const char*)d_in[0], (const char*)d_in[1], (const char*)d_in[2], (const char*)d_in[3], (const char*)d_in[4], (const char*)d_in[5],
      (const char*)d_in[6], (const char*)d_in[7], (const char*)d_in[8], (const char*)d_in[9], (const char*)d_in[10], (const char*)d_in[11], (const char*)d_in[12],
      (const char*)d_in[13], (char*)d_out, (char*)d_ws);
}
```

```cpp
#include <hip/hip_runtime.h>
#include <hip/hip_bf16.h>

typedef unsigned short u16;
typedef unsigned long long u64;
typedef __attribute__((ext_vector_type(8))) __bf16 bf16x8;
typedef __attribute__((ext_vector_type(2))) __bf16 bf16x2;
typedef __attribute__((ext_vector_type(8))) _Float16 f16x8;
typedef __attribute__((ext_vector_type(2))) _Float16 f16x2;
typedef __attribute__((ext_vector_type(4))) short s16x4;
typedef __attribute__((ext_vector_type(8))) short s16x8;
typedef __attribute__((ext_vector_type(4))) float f32x4;
typedef __attribute__((ext_vector_type(2))) float f32x2;
typedef __attribute__((ext_vector_type(16))) float f32x16;
typedef __attribute__((ext_vector_type(4))) unsigned u32x4;
typedef __attribute__((ext_vector_type(2))) unsigned u32x2;
#define LAS __attribute__((address_space(3)))
#define GAS __attribute__((address_space(1)))
typedef LAS char* lptr;
#define DI __device__ __forceinline__
#define SBAR() __builtin_amdgcn_sched_barrier(0)
#define PIN(x) asm volatile("" : "+v"(x))
#define RLX_AGENT __ATOMIC_RELAXED, __HIP_MEMORY_SCOPE_AGENT

constexpr int ROWB = 1152;
constexpr int KV_V = 1216, KV_REC = 2496, KV_CHUNK = 20480;
constexpr unsigned WS_EPOCH = 0, WS_KVF = 256, WS_UF = 8704, WS_YF = 12288, WS_OF = 16384, WS_KV = 32768, WS_O2 = WS_KV + 8 * KV_CHUNK;
constexpr unsigned WS_UT = WS_O2 + 2048 * ROWB, WS_YT = WS_UT + 2048 * ROWB, WS_END = WS_YT + 2048 * ROWB;
constexpr int L_KV = 0, L_KC = 40960, KC_COPY = 4160, KC_CH = 20736, L_QX = 23552;
constexpr int L_TAB = 128512, TAB_W = 8704, L_U = 132608, U_CH = 7680, L_WO = L_KC, L_OX = L_U, L_PART = 0, L_MISC = 163328;
constexpr int M_TAG = 0, M_KMAX = 4, M_GO = 8, M_GO2 = 12, M_CNT = 16, M_N2 = 20, M_OCNT = 24, M_RDY = 32, M_WOR = 48, M_DONE = 64, M_OXR = 96, M_QL = 128, M_QF = 144, M_HS = 160, M_R0 = 192;
constexpr int QS = 17408, L_QR = 23552, L_KW = L_QR + 2 * QS, KW_A = 27648, L_KX = L_KW + 2 * KW_A;
constexpr unsigned EP_STEP = 0x9E3779B9u;
constexpr float LOG2E = 1.4426950408889634f;
constexpr float QSCALE = 0.23570226039551584f * LOG2E;

#define STAMP_DECL
#define STAMP_AT(idx)
#define STAMP_FLUSH(kid)

DI unsigned pk2(float lo, float hi) { f32x2 v = {lo, hi}; bf16x2 b = __builtin_convertvector(v, bf16x2); return __builtin_bit_cast(unsigned, b); }
DI unsigned pkh(f32x2 v) { f16x2 b = __builtin_convertvector(v, f16x2); return __builtin_bit_cast(unsigned, b); }
DI float bflo(unsigned v) { return __uint_as_float(v << 16); }
DI float bfhi(unsigned v) { return __uint_as_float(v & 0xffff0000u); }
DI float sq2(unsigned v) { return bflo(v) * bflo(v) + bfhi(v) * bfhi(v); }

DI void glds16(const void* sbase, unsigned voff, unsigned lds_dst) {
  unsigned sv;
  asm volatile("s_mov_b32 %0, m0\n\ts_mov_b32 m0, %3\n\ts_nop 0\n\tglobal_load_lds_dwordx4 %1, %2\n\ts_mov_b32 m0, %0"
               : "=&s"(sv) : "v"(voff), "s"(sbase), "s"(lds_dst) : "memory");
}
DI void glds16_8lanes(const void* sbase, unsigned voff, unsigned lds_dst) {
  unsigned sv;
  asm volatile("s_mov_b32 %0, m0\n\ts_mov_b32 m0, %3\n\ts_mov_b64 exec, 0xff\n\tglobal_load_lds_dwordx4 %1, %2\n\ts_mov_b64 exec, -1\n\ts_mov_b32 m0, %0"
               : "=&s"(sv) : "v"(voff), "s"(sbase), "s"(lds_dst) : "memory");
}
template <int N> DI void wait_vm_bar() { asm volatile("s_waitcnt vmcnt(%0) lgkmcnt(0)\n\ts_barrier" ::"i"(N) : "memory"); }
DI void bar() { asm volatile("s_waitcnt lgkmcnt(0)\n\ts_barrier" ::: "memory"); }
DI void bar_raw() { asm volatile("s_barrier" ::: "memory"); }
DI bf16x8 lds_b128(lptr p) { return *(const LAS bf16x8*)p; }
DI u32x4 lds_u128(lptr p) { return *(const LAS u32x4*)p; }
DI s16x4 lds_tr(lptr p) { return __builtin_amdgcn_ds_read_tr16_b64_v4i16((LAS s16x4*)p); }
DI bf16x8 cat8(s16x4 a, s16x4 b) { s16x8 v = __builtin_shufflevector(a, b, 0, 1, 2, 3, 4, 5, 6, 7); return __builtin_bit_cast(bf16x8, v); }
#define MFMA16(a, b, c) __builtin_amdgcn_mfma_f32_16x16x32_bf16(a, b, c, 0, 0, 0)
#define MFMA32(a, b, c) __builtin_amdgcn_mfma_f32_32x32x16_bf16(a, b, c, 0, 0, 0)
#define MFMA32H(a, b, c) __builtin_amdgcn_mfma_f32_32x32x16_f16(a, b, c, 0, 0, 0)
#define MFMA32K8(a, b, c) __builtin_amdgcn_mfma_f32_32x32x8bf16_1k(a, b, c, 0, 0, 0)
#define EX(v) __builtin_amdgcn_exp2f(v)
#define PB(P) __builtin_bit_cast(bf16x8, P)

template <int CTRL> DI float dpp_mov(float v) { return __uint_as_float(__builtin_amdgcn_update_dpp(0u, __float_as_uint(v), CTRL, 0xf, 0xf, true)); }
DI float row_max(float v) {
  v = fmaxf(v, dpp_mov<0xB1>(v)); v = fmaxf(v, dpp_mov<0x4E>(v)); v = fmaxf(v, dpp_mov<0x141>(v)); v = fmaxf(v, dpp_mov<0x140>(v));
  return v;
}
DI float wave_max(float v) {
  v = row_max(v);
  auto r1 = __builtin_amdgcn_permlane16_swap(__float_as_uint(v), __float_as_uint(v), false, false);
  v = fmaxf(__uint_as_float(r1[0]), __uint_as_float(r1[1]));
  auto r2 = __builtin_amdgcn_permlane32_swap(__float_as_uint(v), __float_as_uint(v), false, false);
  return fmaxf(__uint_as_float(r2[0]), __uint_as_float(r2[1]));
}
DI float sum_rows(float v) {
  auto r1 = __builtin_amdgcn_permlane16_swap(__float_as_uint(v), __float_as_uint(v), false, false);
  v = __uint_as_float(r1[0]) + __uint_as_float(r1[1]);
  auto r2 = __builtin_amdgcn_permlane32_swap(__float_as_uint(v), __float_as_uint(v), false, false);
  return __uint_as_float(r2[0]) + __uint_as_float(r2[1]);
}
DI float other_half(float v) { auto rr = __builtin_amdgcn_permlane32_swap(__float_as_uint(v), __float_as_uint(v), false, false); return (threadIdx.x & 32) ? __uint_as_float(rr[0]) : __uint_as_float(rr[1]); }
DI bool spin_expired(u64 t0) { __builtin_amdgcn_s_sleep(4); return __builtin_amdgcn_s_memrealtime() - t0 > 400000ull; }
struct CM { f32x2 e, r; };
DI CM mk(f32x2 e) { return CM{e, f32x2{-e[1], e[0]}}; }
DI f32x2 cmul(f32x2 a, const CM& m) { return __builtin_elementwise_fma(f32x2{a[1], a[1]}, m.r, f32x2{a[0], a[0]} * m.e); }
DI f32x2 cexp2(float lg, float rev) { rev -= __builtin_rintf(rev); const float e = EX(lg); return f32x2{e * __builtin_amdgcn_cosf(rev), e * __builtin_amdgcn_sinf(rev)}; }

#define SPIN_SLEEP 8
DI unsigned lds_ld(lptr p) { return *(const volatile LAS unsigned*)p; }
DI void lds_st(lptr p, unsigned v) { *(volatile LAS unsigned*)p = v; }
DI void cfence() { asm volatile("" ::: "memory"); }
template <int SL = SPIN_SLEEP> DI void lds_wait(lptr p, unsigned need) { while (!__all(lds_ld(p) >= need)) __builtin_amdgcn_s_sleep(SL); cfence(); }
DI int rsw(int r) { return (r & ~7) | ((r & 1) << 2) | ((r >> 1) & 3); }
DI void tr8(const u32x4 (&r)[8], u32x4 (&o)[8]) {
#pragma unroll
  for (int k = 0; k < 8; ++k)
#pragma unroll
    for (int m = 0; m < 4; ++m) o[k][m] = __builtin_amdgcn_perm(r[2 * m + 1][k >> 1], r[2 * m][k >> 1], (k & 1) ? 0x07060302u : 0x05040100u);
}
#define EXP 0
#define TU_AFTER 10
#define QSL 0
#define CONV_S3 0
#define CONV_SLEEP 0
#define HPRIO 2

__global__ __launch_bounds__(768) void mega_kernel(const char* __restrict__ u, const char* __restrict__ Are, const char* __restrict__ Aim, const char* __restrict__ Bre,
    const char* __restrict__ Bim, const char* __restrict__ Cre, const char* __restrict__ Cim, const char* __restrict__ ldt, const char* __restrict__ Dsk,
    const char* __restrict__ x, const char* __restrict__ wq, const char* __restrict__ wk, const char* __restrict__ wv, const char* __restrict__ wo,
    char* out, char* ws) {
  __shared__ __attribute__((aligned(16))) char smem[163840];
  const lptr lds = (lptr)smem, misc = lds + L_MISC;
  const unsigned lds0 = (unsigned)(size_t)lds;
  const int tid = threadIdx.x, lane = tid & 63, w = __builtin_amdgcn_readfirstlane(tid >> 6);
  const int b = blockIdx.x, xcd = b & 7, j = b >> 3, rb = xcd * 4 + (j & 3), hg = j >> 2;
  const int c = lane & 31, h = lane >> 5, r16 = lane & 15, g = lane >> 4, q4 = (lane & 15) >> 2, p4 = lane & 3;
  const __amdgpu_buffer_rsrc_t wsr = __builtin_amdgcn_make_buffer_rsrc(ws, 0, WS_END, 0x00020000);
  const f32x16 zero = {};
  const bool ssm = j < 18, kvp = j >= 24;
  STAMP_DECL; STAMP_AT(0);
  asm volatile("" :: "s"(u), "s"(Are), "s"(Aim), "s"(Bre), "s"(Bim), "s"(Cre), "s"(Cim), "s"(ldt), "s"(Dsk), "s"(x), "s"(wq), "s"(wk), "s"(wv), "s"(wo), "s"(out), "s"(ws));

  if (w < 8) {
    const int hd = w & 3, kh = w >> 2;
    if (ssm) __builtin_amdgcn_s_setprio(3);
    if (w == 0) lds_st(misc + lane * 4, 0u);
    unsigned ep7 = 0;
    if (w == 7) ep7 = __hip_atomic_load((GAS unsigned*)(ws + WS_EPOCH), RLX_AGENT);
    u64 fv = 0;
    if (w >= 4) {
      const int iw = w - 4, rot = (2 * (hg + (j & 3))) % 9;
      unsigned xo[2], wo_[3];
#pragma unroll
      for (int i = 0; i < 2; ++i) { const int xrow = 16 * iw + 8 * i + (lane >> 3); xo[i] = xrow * ROWB + (((lane & 7) ^ ((xrow >> 1) & 7)) * 16); }
#pragma unroll
      for (int i = 0; i < 3; ++i) { const int cc = (i < 2 ? 128 * iw + 64 * i : 512) + lane; wo_[i] = rsw(cc / 9) * ROWB + (cc % 9) * 16; }
      const char* xb = x + (size_t)rb * 64 * ROWB;
      const char* wb = wq + hg * 144;
      const lptr qlw = misc + M_QL + iw * 4;
      bar();
      int st = rot, slot = 0;
#pragma unroll 1
      for (int s = 0; s < 9; ++s) {
        if (s == 2 && kvp) lds_wait<2>(misc + M_HS + p4 * 4, 2);
        if (s >= 5) lds_wait<1>(misc + M_QF + p4 * 4, s - 4);
        const unsigned dst = lds0 + L_QR + slot * QS;
        const char* xs = xb + st * 128; const char* wsrc = wb + (size_t)st * 64 * ROWB;
        glds16(xs, xo[0], dst + iw * 2048); glds16(xs, xo[1], dst + iw * 2048 + 1024);
        glds16(wsrc, wo_[0], dst + 8192 + iw * 2048); glds16(wsrc, wo_[1], dst + 8192 + iw * 2048 + 1024);
        if (iw == 0) { glds16(wsrc, wo_[2], dst + 16384); asm volatile("s_waitcnt vmcnt(10)" ::: "memory"); }
        else asm volatile("s_waitcnt vmcnt(8)" ::: "memory");
        lds_st(qlw, s > 0 ? s - 1 : 0);
        st = st == 8 ? 0 : st + 1; slot = slot == 4 ? 0 : slot + 1;
      }
      STAMP_AT(1);
      if (w == 7) fv = __hip_atomic_load((GAS u64*)(ws + WS_KVF + lane * 128), RLX_AGENT);
      if (w == 4 || w == 7) asm volatile("s_waitcnt vmcnt(5)" ::: "memory"); else asm volatile("s_waitcnt vmcnt(4)" ::: "memory");
      lds_st(qlw, 8u);
      if (w == 7) asm volatile("s_waitcnt vmcnt(1)" ::: "memory"); else asm volatile("s_waitcnt vmcnt(0)" ::: "memory");
      lds_st(qlw, 9u);
      if (w == 7) {
        const unsigned tag = ep7 + EP_STEP;
        const u64 t0 = __builtin_amdgcn_s_memrealtime();
        while (!__all((unsigned)(fv >> 32) == tag) && !spin_expired(t0)) fv = __hip_atomic_load((GAS u64*)(ws + WS_KVF + lane * 128), RLX_AGENT);
        __builtin_amdgcn_fence(__ATOMIC_ACQUIRE, "wavefront");
        lds_st(misc + M_GO, 1u);
        const float km = wave_max(__uint_as_float((unsigned)fv));
        if (lane == 0) *(LAS u32x2*)(misc + M_TAG) = u32x2{tag, __float_as_uint(km)};
      } else lds_wait<1>(misc + M_GO, 1);
      STAMP_AT(2);
      const int il = tid - 256, kc = il < 160 ? il : 159;
      u32x4 c0[5];
      const u32x4 k0 = __builtin_amdgcn_raw_buffer_load_b128(wsr, WS_KV + 7 * KV_CHUNK + (6 + (kc >= 80)) * KV_REC + KV_V + (kc >= 80 ? kc - 80 : kc) * 16, 0, 16);
#pragma unroll
      for (int i = 0; i < 5; ++i) c0[i] = __builtin_amdgcn_raw_buffer_load_b128(wsr, WS_KV + il * 16 + i * 4096, 0, 16);
      *(LAS u32x4*)(lds + L_KV + KV_CHUNK + kc * 16) = k0;
#pragma unroll
      for (int i = 0; i < 5; ++i) *(LAS u32x4*)(lds + L_KV + il * 16 + i * 4096) = c0[i];
      cfence(); lds_st(misc + M_R0 + iw * 4, 1u);
    } else {
      lptr xa[2];
#pragma unroll
      for (int ks = 0; ks < 2; ++ks) xa[ks] = lds + L_QR + (16 * w + r16) * 128 + (((4 * ks + g) ^ ((r16 >> 1) & 7)) * 16);
      const lptr wa = lds + L_QR + 8192 + (8 * g + 2 * q4) * 144 + 8 * p4;
      const lptr qlr = misc + M_QL + p4 * 4, qfw = misc + M_QF + w * 4;
      f32x4 acc[5] = {};
      if (kvp) {
        const int xrow = 8 * w + (lane >> 3);
        const unsigned xo = xrow * ROWB + (((lane & 7) ^ ((xrow >> 1) & 7)) * 16);
        const char* xb = x + (size_t)(xcd * 8 + (j - 24)) * 32 * ROWB;
#pragma unroll
        for (int i = 0; i < 9; ++i) glds16(xb + i * 128, xo, lds0 + L_KX + i * 4096 + w * 1024);
      }
      bar();
      if (kvp) { asm volatile("s_waitcnt vmcnt(0)" ::: "memory"); lds_st(misc + M_HS + 16 + w * 4, 1u); }
      bf16x8 xf[2][2], wf[2][2][5];
      lds_wait<1>(qlr, 1);
      STAMP_AT(1);
#pragma unroll
      for (int s = 0; s < 10; ++s) {
        const int so = (s % 5) * QS, bf = s & 1;
        unsigned ql = 0;
#pragma unroll
        for (int i = 0; i < 10; ++i) {
          const int ks = i / 5, t = i % 5;
          if (s < 9) {
            if (t == 0) xf[bf][ks] = lds_b128(xa[ks] + so);
            wf[bf][ks][t] = cat8(lds_tr(wa + so + ks * 32 * 144 + 32 * t), lds_tr(wa + so + ks * 32 * 144 + 32 * t + 144));
            if (i == 9) { cfence(); lds_st(qfw, s + 1); ql = lds_ld(qlr); }
          }
          if (s > 0) { acc[t] = MFMA16(wf[bf ^ 1][ks][t], xf[bf ^ 1][ks], acc[t]); PIN(acc[t]); }
          SBAR();
        }
        if (s < 8 && !__all(ql >= (unsigned)(s + 2))) lds_wait<1>(qlr, s + 2);
        cfence(); SBAR();
      }
      lds_wait<1>(misc + M_QF + p4 * 4, 6);
#pragma unroll
      for (int t = 0; t < 5; ++t)
#pragma unroll
        for (int e = 0; e < 2; ++e) {
          const int n = 16 * t + 4 * g + 2 * e, hh = n / 18;
          if (n < 72) *(LAS unsigned*)(lds + L_QX + (hh * 64 + 16 * w + r16) * 48 + (n - 18 * hh) * 2) = pk2(acc[t][2 * e] * QSCALE, acc[t][2 * e + 1] * QSCALE);
        }
      cfence(); lds_st(qfw, 10u);
      STAMP_AT(2);
    }
    lds_wait<1>(misc + M_QF + p4 * 4, 10);
    const u32x4 qau = lds_u128(lds + L_QX + (hd * 64 + 32 * kh + c) * 48 + 16 * h);
    const u32x4 qbu = lds_u128(lds + L_QX + (hd * 64 + 32 * (kh ^ 1) + c) * 48 + 16 * h);
    const unsigned q2lo = *(const LAS unsigned*)(lds + L_QX + (hd * 64 + ((lane + 32 * kh) & 63)) * 48 + 32);
    float n2;
    {
      const float pa = sq2(qau[0]) + sq2(qau[1]) + sq2(qau[2]) + sq2(qau[3]), pb = sq2(qbu[0]) + sq2(qbu[1]) + sq2(qbu[2]) + sq2(qbu[3]);
      auto rr = __builtin_amdgcn_permlane32_swap(__float_as_uint(pa), __float_as_uint(pb), false, false);
      n2 = __uint_as_float(rr[0]) + __uint_as_float(rr[1]) + sq2(q2lo);
    }
    const bf16x8 q1a = PB(qau), q1b = PB(qbu);
    lptr vb = lds + L_KV + kh * KV_REC + h * 304 + (c < 18 ? c : 18) * 16;
    lptr kb = lds + L_KV + kh * KV_REC + KV_V + lane * 16;
    lptr k2b = lds + L_KV + kh * KV_REC + KV_V + 1024 + c * 8;
    const lptr rdyp = misc + M_RDY + p4 * 4, donep = misc + M_DONE + w * 4, pdp = misc + M_DONE + (w ^ 4) * 4;
    lds_wait<1>(misc + M_R0 + p4 * 4, 1);
    __builtin_amdgcn_s_setprio(1);
    STAMP_AT(3);
    const float kmx = *(const LAS float*)(misc + M_KMAX);
    bf16x8 kf = lds_b128(lds + L_KV + KV_CHUNK + kh * 1280 + lane * 16);
    s16x4 kf2 = *(const LAS s16x4*)(lds + L_KV + KV_CHUNK + kh * 1280 + 1024 + c * 8);
    cfence(); lds_st(donep, 1u);
    const float bound = __builtin_amdgcn_sqrtf(n2 * kmx) * 1.01f;
    const bool fast = __all(bound <= 40.f);
    f32x16 Oa = zero, Ob = zero;
    float ma = 0.f, mb = 0.f;
    if (fast) {
      const unsigned nm = pk2(-bound, 0.f);
      const u32x2 qa = h ? u32x2{0u, 0u} : u32x2{q2lo, nm}, qb = h ? u32x2{q2lo, nm} : u32x2{0u, 0u};
      const s16x4 q2a = __builtin_bit_cast(s16x4, qa), q2b = __builtin_bit_cast(s16x4, qb);
      f32x16 Aa, Ab, Ba, Bb;
      Aa = MFMA32K8(kf2, q2a, zero); Ab = MFMA32K8(kf2, q2b, zero);
      Aa = MFMA32(kf, q1a, Aa); Ab = MFMA32(kf, q1b, Ab);
      bf16x8 vf0 = lds_b128(vb), vf1 = lds_b128(vb + 608);
      kf = lds_b128(kb); kf2 = *(const LAS s16x4*)k2b;
      u32x4 p0, p1;
#define GRP(MF, RD, S, i, P, jj) do { float e0 = EX(S[i]), e1 = EX(S[i + 1]); SBAR(); MF; RD; SBAR(); float e2 = EX(S[i + 2]), e3 = EX(S[i + 3]); P[jj] = pk2(e0, e1); P[jj + 1] = pk2(e2, e3); PIN(P); SBAR(); } while (0)
#define HALF1(Ca, Cb, Na, RD) do { \
      GRP(Na = MFMA32K8(kf2, q2a, zero), , Ca, 4, p0, 2); \
      GRP(Oa = MFMA32(vf0, PB(p0), Oa), RD, Ca, 8, p1, 0); \
      GRP(Na = MFMA32(kf, q1a, Na), , Ca, 12, p1, 2); \
      GRP(Oa = MFMA32(vf1, PB(p1), Oa), , Cb, 0, p0, 0); } while (0)
#define HALF2(Cb, Na, Nb, off) do { \
      GRP(Nb = MFMA32K8(kf2, q2b, zero), kf2 = *(const LAS s16x4*)(k2b + (off)), Cb, 4, p0, 2); \
      GRP(Ob = MFMA32(vf0, PB(p0), Ob), vf0 = lds_b128(vb + (off)), Cb, 8, p1, 0); \
      GRP(Nb = MFMA32(kf, q1b, Nb), kf = lds_b128(kb + (off)), Cb, 12, p1, 2); \
      GRP(Ob = MFMA32(vf1, PB(p1), Ob), vf1 = lds_b128(vb + (off) + 608), Na, 0, p0, 0); } while (0)
      p0[0] = pk2(EX(Aa[0]), EX(Aa[1])); p0[1] = pk2(EX(Aa[2]), EX(Aa[3])); p0[2] = 0; p0[3] = 0; p1 = p0;
      STAMP_AT(11);
      for (int p = 0; p < 8; ++p) {
        unsigned rdy;
        HALF1(Aa, Ab, Ba, ); HALF2(Ab, Ba, Bb, 2 * KV_REC);
        HALF1(Ba, Bb, Aa, ); HALF2(Bb, Aa, Ab, 4 * KV_REC);
        HALF1(Aa, Ab, Ba, ); HALF2(Ab, Ba, Bb, 6 * KV_REC);
        cfence(); lds_st(donep, p + 2); SBAR();
        unsigned pd;
        HALF1(Ba, Bb, Aa, rdy = lds_ld(rdyp); pd = lds_ld(pdp));
        if (__all(pd < (unsigned)(p + 2))) __builtin_amdgcn_s_setprio(0); else __builtin_amdgcn_s_setprio(1);
        const int adv = (p & 1) ? -KV_CHUNK : KV_CHUNK;
        vb += adv; kb += adv; k2b += adv;
        const unsigned need = p < 7 ? p + 2 : 0;
        if (__builtin_expect(!__all(rdy >= need), 0)) lds_wait(rdyp, need);
        cfence(); SBAR();
        HALF2(Bb, Aa, Ab, 0);
      }
    } else {
      const u32x2 qa = h ? u32x2{0u, 0u} : u32x2{q2lo, 0u}, qb = h ? u32x2{q2lo, 0u} : u32x2{0u, 0u};
      const s16x4 q2a = __builtin_bit_cast(s16x4, qa), q2b = __builtin_bit_cast(s16x4, qb);
      ma = -INFINITY; mb = -INFINITY;
      for (int i = 0; i < 32; ++i) {
        if (i && (i & 3) == 0) lds_wait(rdyp, (i >> 2) + 1);
        const int off = ((i >> 2) & 1) * KV_CHUNK + (i & 3) * 2 * KV_REC;
        f32x16 Sa = MFMA32K8(kf2, q2a, zero), Sb = MFMA32K8(kf2, q2b, zero);
        Sa = MFMA32(kf, q1a, Sa); Sb = MFMA32(kf, q1b, Sb);
        kf = lds_b128(kb + off); kf2 = *(const LAS s16x4*)(k2b + off);
        const bf16x8 vf0 = lds_b128(vb + off), vf1 = lds_b128(vb + off + 608);
        if ((i & 3) == 3) { cfence(); lds_st(donep, (i >> 2) + 2); }
        float ta = ma, tb = mb;
#pragma unroll
        for (int r = 0; r < 16; ++r) { ta = fmaxf(ta, Sa[r]); tb = fmaxf(tb, Sb[r]); }
        ta = fmaxf(ta, other_half(ta)); tb = fmaxf(tb, other_half(tb));
        const float fa = EX(ma - ta), fb = EX(mb - tb);
        ma = ta; mb = tb;
        u32x4 pp[4];
#pragma unroll
        for (int r = 0; r < 8; ++r) {
          Oa[2 * r] *= fa; Oa[2 * r + 1] *= fa; Ob[2 * r] *= fb; Ob[2 * r + 1] *= fb;
          pp[r >> 2][r & 3] = pk2(EX(Sa[2 * r] - ma), EX(Sa[2 * r + 1] - ma));
          pp[2 + (r >> 2)][r & 3] = pk2(EX(Sb[2 * r] - mb), EX(Sb[2 * r + 1] - mb));
        }
        Oa = MFMA32(vf0, PB(pp[0]), Oa); Ob = MFMA32(vf0, PB(pp[2]), Ob);
        Oa = MFMA32(vf1, PB(pp[1]), Oa); Ob = MFMA32(vf1, PB(pp[3]), Ob);
      }
    }
    STAMP_AT(4);
    __builtin_amdgcn_s_setprio(0);
    lds_wait<2>(misc + M_WOR + p4 * 4, 1);
    {
      const lptr ox = lds + L_OX + w * 3072 + lane * 16;
      *(LAS f32x4*)(ox) = f32x4{Ob[0], Ob[1], Ob[2], Ob[3]};
      *(LAS f32x4*)(ox + 1024) = f32x4{Ob[4], Ob[5], Ob[6], Ob[7]};
      *(LAS f32x4*)(ox + 2048) = f32x4{Ob[8], Ob[9], Ob[10], mb};
      cfence(); lds_st(misc + M_OXR + w * 4, 1u);
    }
    lds_wait<2>(misc + M_OXR + (w ^ 4) * 4, 1);
    STAMP_AT(7);
    {
      const lptr ox = lds + L_OX + (w ^ 4) * 3072 + lane * 16;
      const f32x4 t0 = *(const LAS f32x4*)(ox), t1 = *(const LAS f32x4*)(ox + 1024), t2 = *(const LAS f32x4*)(ox + 2048);
      const float mm = fmaxf(ma, t2[3]);
      float fa = EX(ma - mm), fo = EX(t2[3] - mm);
      const float inv = 1.f / (Oa[10] * fa + t2[2] * fo);
      fa *= inv; fo *= inv;
      const int tok = 32 * kh + c, gm = 9 * hg + 2 * hd + h, g8 = 9 * hg + 8;
      const unsigned ob = WS_O2 + ((rb * 4 + (tok >> 4)) * 72 * 16 + (tok & 15)) * 16;
      const u32x4 v = {pk2(Oa[0] * fa + t0[0] * fo, Oa[1] * fa + t0[1] * fo), pk2(Oa[2] * fa + t0[2] * fo, Oa[3] * fa + t0[3] * fo),
                       pk2(Oa[4] * fa + t1[0] * fo, Oa[5] * fa + t1[1] * fo), pk2(Oa[6] * fa + t1[2] * fo, Oa[7] * fa + t1[3] * fo)};
      __builtin_amdgcn_raw_buffer_store_b128(v, wsr, ob + gm * 256, 0, 16);
      if (h == 0) __builtin_amdgcn_raw_buffer_store_b32(pk2(Oa[8] * fa + t2[0] * fo, Oa[9] * fa + t2[1] * fo), wsr, ob + g8 * 256 + hd * 4, 0, 16);
      const unsigned tg = *(const LAS unsigned*)(misc + M_TAG);
      asm volatile("s_waitcnt vmcnt(0)" ::: "memory");
      if (lane == 0) {
        if (__hip_atomic_fetch_add((LAS unsigned*)(misc + M_OCNT), 1u, __ATOMIC_RELAXED, __HIP_MEMORY_SCOPE_WORKGROUP) == 7u)
          __hip_atomic_store((GAS unsigned*)(ws + WS_OF) + (rb * 8 + hg) * 16, tg, RLX_AGENT);
      }
      STAMP_AT(8);
    }
  } else {
    const int hw = w - 8, hl = tid - 512;
    __builtin_amdgcn_s_setprio(HPRIO);
    const unsigned ep = __hip_atomic_load((GAS unsigned*)(ws + WS_EPOCH), RLX_AGENT);
#define GET_TAG(t) unsigned t; asm volatile("v_add_u32 %0, %2, %1" : "=v"(t) : "v"(ep), "s"(EP_STEP))
    const int d = xcd * 72 + j * 4 + hw;
    unsigned woff[11];
    auto wo_offsets = [&]() {
#pragma unroll
      for (int i = 0; i < 11; ++i) {
        const int cc = 64 * i + (i < 10 ? lane : lane & 7), row = cc / 9, kk = rsw(row), cl = kk >> 3, e = kk & 7;
        woff[i] = (cl < 8 ? 18 * (cl >> 1) + 4 * (cl & 1) + (e & 3) + 2 * (e & 4) : 18 * (e >> 1) + 16 + (e & 1)) * ROWB + (cc - 9 * row) * 16;
        PIN(woff[i]);
      }
    };
    unsigned dsk = 0;
    f32x16 kacc = zero; float amax = 0.f; int ce = 0;
    if (ssm) {
      const unsigned po = d * 256 + lane * 4;
      const unsigned vdt = *(const u16*)(ldt + d * 2);
      const unsigned vAr = *(const unsigned*)(Are + po), vAi = *(const unsigned*)(Aim + po), vBr = *(const unsigned*)(Bre + po), vBi = *(const unsigned*)(Bim + po);
      const unsigned vCr = *(const unsigned*)(Cre + po), vCi = *(const unsigned*)(Cim + po);
      dsk = *(const u16*)(Dsk + d * 2);
      SBAR(); bar_raw(); SBAR();
      wo_offsets(); SBAR();
      const float dt = bflo(pk2(expf(bflo(vdt)), 0.f));
      float a2[2], rv[2]; f32x2 cc[2];
#pragma unroll
      for (int bt = 0; bt < 2; ++bt) {
        const float ar = bt ? bfhi(vAr) : bflo(vAr), ai = bt ? bfhi(vAi) : bflo(vAi), br = dt * (bt ? bfhi(vBr) : bflo(vBr)), bi = dt * (bt ? bfhi(vBi) : bflo(vBi));
        const float cr = bt ? bfhi(vCr) : bflo(vCr), ci = bt ? bfhi(vCi) : bflo(vCi);
        a2[bt] = dt * ar * LOG2E; rv[bt] = dt * ai * 0.15915494309189535f;
        cc[bt] = f32x2{cr * br - ci * bi, -(cr * bi + ci * br)};
      }
      amax = wave_max(fmaxf(a2[0], a2[1]));
      const float cmax = wave_max(fmaxf(fmaxf(fabsf(cc[0][0]), fabsf(cc[0][1])), fmaxf(fabsf(cc[1][0]), fabsf(cc[1][1]))));
      ce = __builtin_amdgcn_frexp_expf(cmax);
      const lptr tw = lds + L_TAB + hw * TAB_W + lane * 4;
      const lptr tra = lds + L_TAB + hw * TAB_W + (c < 20 ? c : 20) * 272 + 16 * h, trb = lds + L_TAB + hw * TAB_W + c * 272 + 16 * h;
#define TW(row, v) (*(LAS unsigned*)(tw + (row) * 272) = pkh(v))
      STAMP_AT(1);
#pragma unroll
      for (int bt = 0; bt < 2; ++bt) {
        const float an = a2[bt] - amax;
        const CM E1 = mk(cexp2(an, -rv[bt])), E256 = mk(cexp2(256.f * an, -256.f * rv[bt])), E8 = mk(cexp2(8.f * an, 8.f * rv[bt]));
        const f32x2 x0 = {__builtin_amdgcn_ldexpf(cc[bt][0], 14 - ce), __builtin_amdgcn_ldexpf(cc[bt][1], 14 - ce)};
        const CM E2 = mk(cmul(E1.e, E1)), E4 = mk(cmul(E2.e, E2)), E512 = mk(cmul(E256.e, E256));
        {
          const f32x2 x1 = cmul(x0, E1), x2 = cmul(x0, E2), y0 = cmul(x0, E256), z0 = cmul(x0, E512), x3 = cmul(x1, E2);
          TW(0, x0); TW(1, x1); TW(2, x2); TW(3, x3);
          const f32x2 z1 = cmul(y0, E512), z2 = cmul(z0, E512), y1 = cmul(y0, E2);
          TW(4, cmul(x0, E4)); TW(5, cmul(x1, E4)); TW(6, cmul(x2, E4)); TW(7, cmul(x3, E4));
          const f32x2 z3 = cmul(z1, E512), z4 = cmul(z2, E512);
          TW(8, y0); TW(9, y1); TW(10, cmul(y0, E4)); TW(11, cmul(y1, E4));
          TW(12, z0); TW(13, cmul(z0, E4)); TW(14, z1); TW(15, cmul(z1, E4));
          TW(16, z2); TW(17, z3); TW(18, z4); TW(19, cmul(z3, E512)); TW(20, cmul(z4, E512));
        }
        f16x8 af[8];
#pragma unroll
        for (int ks = 0; ks < 8; ++ks) af[ks] = *(const LAS f16x8*)(tra + 32 * ks);
        if (bt == 0) STAMP_AT(6); else STAMP_AT(8);
        const CM E16 = mk(cmul(E8.e, E8)), E32 = mk(cmul(E16.e, E16));
        f32x2 y[4]; y[0] = f32x2{16384.f, 0.f}; y[1] = E8.e * 16384.f; y[2] = E16.e * 16384.f; y[3] = cmul(y[1], E16);
#pragma unroll
        for (int i = 0; i < 8; ++i) {
#pragma unroll
          for (int k = 0; k < 4; ++k) { TW(4 * i + k, y[k]); if (i < 7) y[k] = cmul(y[k], E32); }
        }
        f16x8 bfr[8];
#pragma unroll
        for (int ks = 0; ks < 8; ++ks) bfr[ks] = *(const LAS f16x8*)(trb + 32 * ks);
        if (bt == 0) STAMP_AT(7); else STAMP_AT(14);
#pragma unroll
        for (int ks = 0; ks < 8; ++ks) kacc = MFMA32H(af[ks], bfr[ks], kacc);
      }
    } else if (kvp) {
      const int t = xcd * 8 + (j - 24), tq = hw & 1, sel = hw >> 1;
      {
        const char* wsrc = (sel ? wv : wk) + tq * 2304;
        unsigned o3[3];
#pragma unroll
        for (int i = 0; i < 3; ++i) { const int cc = 64 * i + lane, row = cc / 3, e = cc - 3 * row; o3[i] = rsw(row) * 36 + (e == 2 ? 20 : 16 * e); }
#pragma unroll
        for (int kb = 0; kb < 5; ++kb)
          if (kb < 4 || tq == 0) {
#pragma unroll
            for (int i = 0; i < 3; ++i) glds16(wsrc + kb * 4608, o3[i], lds0 + L_KW + sel * KW_A + tq * 3072 + kb * 6144 + i * 1024);
          }
      }
      STAMP_AT(6);
      SBAR(); bar_raw(); SBAR();
      asm volatile("s_waitcnt vmcnt(0)" ::: "memory");
      lds_st(misc + M_HS + hw * 4, 1u); lds_wait<1>(misc + M_HS + (lane & 7) * 4, 1);
      STAMP_AT(8);
      f32x4 aM = {0.f, 0.f, 0.f, 0.f}, aR = aM;
      {
        const lptr ma_ = lds + L_KW + sel * KW_A + (8 * g + 2 * q4) * 48 + 8 * p4, ra_ = lds + L_KW + sel * KW_A + (8 * g + 2 * q4) * 48 + 40;
        lptr xa[2];
#pragma unroll
        for (int ks = 0; ks < 2; ++ks) xa[ks] = lds + L_KX + (16 * tq + r16) * 128 + (((4 * ks + g) ^ ((r16 >> 1) & 7)) * 16);
        bf16x8 xf[3], fm[3], fr[3];
        auto rd = [&](int i) { xf[i % 3] = lds_b128(xa[i & 1] + (i >> 1) * 4096); fm[i % 3] = cat8(lds_tr(ma_ + i * 1536), lds_tr(ma_ + i * 1536 + 48)); fr[i % 3] = cat8(lds_tr(ra_ + i * 1536), lds_tr(ra_ + i * 1536 + 48)); };
        rd(0); rd(1);
        if (sel == 0) {
#pragma unroll
          for (int i = 0; i < 18; ++i) { if (i + 2 < 18) rd(i + 2); SBAR(); aM = MFMA16(fm[i % 3], xf[i % 3], aM); aR = MFMA16(fr[i % 3], xf[i % 3], aR); SBAR(); }
        } else {
#pragma unroll
          for (int i = 0; i < 18; ++i) { if (i + 2 < 18) rd(i + 2); SBAR(); aM = MFMA16(xf[i % 3], fm[i % 3], aM); aR = MFMA16(xf[i % 3], fr[i % 3], aR); SBAR(); }
        }
      }
      STAMP_AT(10);
      float n2 = 0.f;
      const int kh_ = t & 1, ti = t >> 1;
      if (sel == 0) {
        const int rk = (ti + 31) & 31;
        char* kd = ws + WS_KV + (rk >> 2) * KV_CHUNK + ((rk & 3) * 2 + kh_) * KV_REC + KV_V;
        const unsigned k01 = pk2(aM[0], aM[1]), k23 = pk2(aM[2], aM[3]), kr = pk2(aR[2], aR[3]);
        __hip_atomic_store((GAS u64*)(kd + (g >> 1) * 512 + (16 * tq + r16) * 16 + (g & 1) * 8), (u64)k01 | ((u64)k23 << 32), RLX_AGENT);
        if (g == 0) __hip_atomic_store((GAS u64*)(kd + 1024 + (16 * tq + r16) * 8), (u64)kr | (0x3f80ull << 32), RLX_AGENT);
        n2 = sq2(k01) + sq2(k23) + (g == 0 ? sq2(kr) : 0.f);
      } else {
        char* vd = ws + WS_KV + (ti >> 2) * KV_CHUNK + ((ti & 3) * 2 + kh_) * KV_REC + tq * 608 + (g & 1) * 304 + (g >> 1) * 8;
        __hip_atomic_store((GAS u64*)(vd + r16 * 16), (u64)pk2(aM[0], aM[1]) | ((u64)pk2(aM[2], aM[3]) << 32), RLX_AGENT);
        if (r16 >= 2 && r16 < 5) __hip_atomic_store((GAS u64*)(vd + (14 + r16) * 16), r16 == 4 ? 0x3f803f803f803f80ull : ((u64)pk2(aR[0], aR[1]) | ((u64)pk2(aR[2], aR[3]) << 32)), RLX_AGENT);
      }
      cfence(); lds_st(misc + M_HS + hw * 4, 2u);
      n2 = row_max(sum_rows(n2));
      if (lane == 0) __hip_atomic_fetch_max((LAS unsigned*)(misc + M_N2), __float_as_uint(n2), __ATOMIC_RELAXED, __HIP_MEMORY_SCOPE_WORKGROUP);
      asm volatile("s_waitcnt vmcnt(0)" ::: "memory");
      GET_TAG(tg);
      if (lane == 0) {
        if (__hip_atomic_fetch_add((LAS unsigned*)(misc + M_CNT), 1u, __ATOMIC_RELAXED, __HIP_MEMORY_SCOPE_WORKGROUP) == 3u)
          __hip_atomic_store((GAS u64*)(ws + WS_KVF + t * 128), ((u64)tg << 32) | lds_ld(misc + M_N2), RLX_AGENT);
      }
      STAMP_AT(1);
      wo_offsets();
    } else {
      {
        const int mw = (xcd * 6 + (j - 18)) * 4 + hw;
        u32x4 ra[2][8];
#pragma unroll
        for (int k = 0; k < 2; ++k) {
          int task = mw + 192 * k; if (task >= 288) task = mw;
          const char* src = u + (size_t)((task & 31) * 64 + (lane >> 3) * 8) * ROWB + (task >> 5) * 128 + (lane & 7) * 16;
#pragma unroll
          for (int e = 0; e < 8; ++e) ra[k][e] = *(const u32x4*)(src + e * ROWB);
        }
        SBAR(); bar_raw(); SBAR();
#pragma unroll
        for (int k = 0; k < 2; ++k) {
          const int task = mw + 192 * k;
          if (task < 288) {
            u32x4 o[8]; tr8(ra[k], o);
#pragma unroll
            for (int e = 0; e < 8; ++e) __builtin_amdgcn_raw_buffer_store_b128(o[e], wsr, WS_UT + ((task >> 5) * 64 + (lane & 7) * 8 + e) * 4096 + (task & 31) * 128 + (lane >> 3) * 16, 0, 16);
          }
        }
        asm volatile("s_waitcnt vmcnt(0)" ::: "memory");
        GET_TAG(tg);
        if (lane < 2 && mw + 192 * lane < 288) __hip_atomic_store((GAS unsigned*)(ws + WS_UF) + mw + 192 * lane, tg, RLX_AGENT);
        STAMP_AT(1);
      }
      wo_offsets();
    }
    GET_TAG(tag);
    u32x4 cv[5];
    const lptr rdyw = misc + M_RDY + hw * 4, donep = misc + M_DONE + (lane & 7) * 4;
    auto fetch = [&](int n) {
#pragma unroll
      for (int i = 0; i < 5; ++i) cv[i] = __builtin_amdgcn_raw_buffer_load_b128(wsr, WS_KV + hl * 16 + i * 4096, n * KV_CHUNK, 16);
    };
    auto stage_now = [&](int p) {
#pragma unroll
      for (int i = 0; i < 5; ++i) *(LAS u32x4*)(lds + L_KV + ((p + 1) & 1) * KV_CHUNK + hl * 16 + i * 4096) = cv[i];
      cfence(); lds_st(rdyw, p + 2);
      if (p < 6) fetch(p + 2);
    };
    auto stage = [&](int p) { lds_wait(donep, p + 1); stage_now(p); };
    int nxt = 0;
    if (ssm) {
      unsigned uf;
      {
        const float ec = (float)(ce - 28), c8 = (float)(8 * c);
        float kv_[12];
#pragma unroll
        for (int r = 0; r < 12; ++r) {
          const int b0 = r < 4 ? r : r < 8 ? 256 + 2 * (r - 4) : 1024 + 256 * (r - 8);
          const int b1 = r < 4 ? 4 + r : r < 8 ? 512 + 256 * ((r - 4) >> 1) + 4 * ((r - 4) & 1) : 2048;
          const float l = c8 + (h ? (float)b1 : (float)b0);
          kv_[r] = kacc[r] * EX(__builtin_fmaf(amax, l, ec));
        }
        const lptr kq = lds + L_TAB + hw * TAB_W;
        *(LAS u32x2*)(kq + (8 * c + 4 * h) * 2) = u32x2{pk2(kv_[0], kv_[1]), pk2(kv_[2], kv_[3])};
        *(LAS unsigned*)(kq + (h ? 384 + 2 * c : 256 + 4 * c) * 2) = pk2(kv_[4], kv_[5]);
        *(LAS unsigned*)(kq + (h ? 448 + 2 * c : 258 + 4 * c) * 2) = pk2(kv_[6], kv_[7]);
        const unsigned k89 = pk2(kv_[8], kv_[9]), kab = pk2(kv_[10], kv_[11]);
        if (h == 0) {
          *(LAS u16*)(kq + (512 + c) * 2) = (u16)k89; *(LAS u16*)(kq + (544 + c) * 2) = (u16)(k89 >> 16);
          *(LAS u16*)(kq + (576 + c) * 2) = (u16)kab; *(LAS u16*)(kq + (608 + c) * 2) = (u16)(kab >> 16);
        } else if (c == 0) *(LAS unsigned*)(kq + 640 * 2) = k89 & 0xffffu;
      }
      STAMP_AT(2);
      unsigned dd[4][6];
      {
        const LAS u16* kq = (const LAS u16*)(lds + L_TAB + hw * TAB_W);
#pragma unroll
        for (int it = 0; it < 4; ++it) {
          const int s0 = 2047 - 8 * (lane + 64 * it);
#pragma unroll
          for (int e = 0; e < 6; ++e) {
            const int sa = s0 - 2 * e, sb = sa - 1;
            const unsigned bits = pk2((float)sa, (float)sb);
            int ia = (int)(bits & 0xffffu) - 0x4280, ib = (int)(bits >> 16) - 0x4280;
            if (it == 3) { ia = sa < 0 ? 641 : sa < 256 ? sa : ia; ib = sb < 0 ? 641 : sb < 256 ? sb : ib; }
            dd[it][e] = (unsigned)kq[ia] | ((unsigned)kq[ib] << 16);
          }
        }
      }
      cfence(); lds_st(misc + M_HS + hw * 4, 1u);
      STAMP_AT(3);
      lds_wait<2>(misc + M_QF + p4 * 4, 10);
      STAMP_AT(4);
      const lptr kc_ = lds + L_KC + hw * KC_CH;
#pragma unroll
      for (int it = 0; it < 4; ++it) {
        unsigned al[5];
#pragma unroll
        for (int e = 0; e < 5; ++e) al[e] = __builtin_amdgcn_alignbit(dd[it][e + 1], dd[it][e], 16);
        const lptr dst = kc_ + (lane + 64 * it) * 16;
        *(LAS u32x4*)(dst) = u32x4{dd[it][0], dd[it][1], dd[it][2], dd[it][3]};
        *(LAS u32x4*)(dst + KC_COPY) = u32x4{al[0], al[1], al[2], al[3]};
        *(LAS u32x4*)(dst + 2 * KC_COPY) = u32x4{dd[it][1], dd[it][2], dd[it][3], dd[it][4]};
        *(LAS u32x4*)(dst + 3 * KC_COPY) = u32x4{al[1], al[2], al[3], al[4]};
      }
      if (lane < 16) *(LAS u32x4*)(kc_ + (lane >> 2) * KC_COPY + 4096 + (lane & 3) * 16) = u32x4{0u, 0u, 0u, 0u};
      lds_wait<1>(misc + M_GO, 1);
      fetch(1);
      uf = __hip_atomic_load((GAS unsigned*)(ws + WS_UF) + (d >> 6) * 32 + c, RLX_AGENT);
      lds_wait<2>(donep, 1); stage_now(0); nxt = 1;
      {
        const u64 t0 = __builtin_amdgcn_s_memrealtime();
        while (!__all(uf == tag) && !spin_expired(t0)) {
          __builtin_amdgcn_s_sleep(8);
          if (nxt < 7 && __all(lds_ld(donep) > (unsigned)nxt)) { cfence(); stage_now(nxt); ++nxt; }
          uf = __hip_atomic_load((GAS unsigned*)(ws + WS_UF) + (d >> 6) * 32 + c, RLX_AGENT);
        }
        __builtin_amdgcn_fence(__ATOMIC_ACQUIRE, "wavefront");
      }
      u32x4 ut[4];
#pragma unroll
      for (int i = 0; i < 4; ++i) ut[i] = __builtin_amdgcn_raw_buffer_load_b128(wsr, WS_UT + d * 4096 + lane * 16 + i * 1024, 0, 16);
      lds_wait<1>(misc + M_HS + p4 * 4, 1);
      const lptr ui = lds + L_U + hw * U_CH;
#pragma unroll
      for (int i = 0; i < 3; ++i) if (lane + 64 * i < 160) *(LAS u32x4*)(ui + (lane + 64 * i) * 16) = u32x4{0u, 0u, 0u, 0u};
#pragma unroll
      for (int i = 0; i < 4; ++i) *(LAS u32x4*)(ui + (32 + 16 * i + (lane >> 2)) * 80 + (lane & 3) * 16) = ut[i];
      STAMP_AT(5);
      f32x16 y0, y1;
      lptr ka = lds + L_KC + hw * KC_CH + (3 - (c & 3)) * KC_COPY + (28 - (c & 28) + 8 * h) * 2 + 30 * 64;
      lptr ua = ui + (c + 1) * 80 + 16 * h + 30 * 80;
#pragma unroll
      for (int ks = 0; ks < 2; ++ks) {
        const int rel = c - 16 * ks - 8 * h;
        u32x4 df;
#pragma unroll
        for (int e = 0; e < 4; ++e) df[e] = (rel >> 1) == e ? dsk << (16 * (rel & 1)) : 0u;
        y0 = MFMA32(PB(df), lds_b128(ua + 80 + 32 * ks), ks ? y0 : zero);
        y1 = MFMA32(PB(df), lds_b128(ua + 33 * 80 + 32 * ks), ks ? y1 : zero);
      }
      bf16x8 an[2], af[2], b0[2], b1[2];
#define CLD(bf_, ko, uo) do { an[bf_] = cat8(*(const volatile LAS s16x4*)(ka + (ko) + 2048), *(const volatile LAS s16x4*)(ka + (ko) + 2048 + 8)); b0[bf_] = lds_b128(ua + (uo)); \
          b1[bf_] = lds_b128(ua + (uo) + 32 * 80); af[bf_] = cat8(*(const volatile LAS s16x4*)(ka + (ko)), *(const volatile LAS s16x4*)(ka + (ko) + 8)); } while (0)
#define CSL() do { SBAR(); if (CONV_SLEEP) __builtin_amdgcn_s_sleep(CONV_SLEEP); SBAR(); } while (0)
#define CMM(bf_) do { y0 = MFMA32(an[bf_], b0[bf_], y0); CSL(); y1 = MFMA32(an[bf_], b1[bf_], y1); CSL(); y1 = MFMA32(af[bf_], b0[bf_], y1); if (CONV_S3) CSL(); } while (0)
      CLD(0, 64, 80);
#pragma unroll 1
      for (int dl = 0; dl < 32; ++dl) {
        const unsigned dn = lds_ld(donep);
        SBAR(); CLD(1, 64 + 32, 80 + 32); SBAR(); CMM(0);
        if (nxt < 7 && __all(dn > (unsigned)nxt)) { cfence(); stage_now(nxt); ++nxt; }
        SBAR(); CLD(0, 0, 0); SBAR(); CMM(1);
        ka -= 64; ua -= 80;
      }
      STAMP_AT(9);
      const lptr yw = ui + (32 + c) * 80 + 8 * h;
#pragma unroll
      for (int i = 0; i < 4; ++i) {
        *(LAS u32x2*)(yw + 16 * i) = u32x2{pk2(y0[4 * i], y0[4 * i + 1]), pk2(y0[4 * i + 2], y0[4 * i + 3])};
        *(LAS u32x2*)(yw + 32 * 80 + 16 * i) = u32x2{pk2(y1[4 * i], y1[4 * i + 1]), pk2(y1[4 * i + 2], y1[4 * i + 3])};
      }
#pragma unroll
      for (int i = 0; i < 4; ++i) __builtin_amdgcn_raw_buffer_store_b128(lds_u128(ui + (32 + 16 * i + (lane >> 2)) * 80 + (lane & 3) * 16), wsr, WS_YT + d * 4096 + lane * 16 + i * 1024, 0, 16);
      asm volatile("s_waitcnt vmcnt(0)" ::: "memory");
      if (lane == 0) __hip_atomic_store((GAS unsigned*)(ws + WS_YF) + d, tag, RLX_AGENT);
      STAMP_AT(10);
    } else {
      lds_wait<4>(misc + M_GO, 1);
      fetch(1);
      stage(0); nxt = 1;
    }
#pragma unroll 1
    for (int hq = 2 * hw; hq < 2 * hw + 2; ++hq) {
      const char* src = wo + hg * 144 + (size_t)hq * 72 * ROWB;
#pragma unroll
      for (int i = 0; i < 10; ++i) glds16(src, woff[i], lds0 + L_WO + hq * 10368 + i * 1024);
      glds16_8lanes(src, woff[10], lds0 + L_WO + hq * 10368 + 10240);
      if (nxt < 7 && __all(lds_ld(donep) > (unsigned)nxt)) { cfence(); stage_now(nxt); ++nxt; }
    }
    STAMP_AT(11);
#pragma unroll 1
    for (; nxt < 7; ++nxt) stage(nxt);
    asm volatile("s_waitcnt vmcnt(0) lgkmcnt(0)" ::: "memory");
    lds_st(misc + M_WOR + hw * 4, 1u);
    STAMP_AT(12);
    if (!ssm && hw < 3) {
      const int task = (xcd * 14 + (j - 18)) * 3 + hw;
      if (task < 288) {
        const u64 t0 = __builtin_amdgcn_s_memrealtime();
        while (!__all(__hip_atomic_load((GAS unsigned*)(ws + WS_YF) + (task >> 5) * 64 + lane, RLX_AGENT) == tag) && !spin_expired(t0)) __builtin_amdgcn_s_sleep(8);
        __builtin_amdgcn_fence(__ATOMIC_ACQUIRE, "wavefront");
        u32x4 ra[8], o[8];
#pragma unroll
        for (int e = 0; e < 8; ++e) ra[e] = __builtin_amdgcn_raw_buffer_load_b128(wsr, WS_YT + ((task >> 5) * 64 + (lane & 7) * 8 + e) * 4096 + (task & 31) * 128 + (lane >> 3) * 16, 0, 16);
        tr8(ra, o);
        char* dst = out + (size_t)((task & 31) * 64 + (lane >> 3) * 8) * (2 * ROWB) + (task >> 5) * 128 + (lane & 7) * 16;
#pragma unroll
        for (int e = 0; e < 8; ++e) *(u32x4*)(dst + e * 2 * ROWB) = o[e];
      }
    }
    STAMP_AT(13);
    lds_wait(misc + M_WOR + p4 * 4, 1);
  }
  const unsigned tag = *(const LAS unsigned*)(misc + M_TAG);
  const int rt = w & 3, kt = w >> 2;
  bf16x8 wf[6][5];
  const lptr wwa = lds + L_WO + (192 * kt + 8 * g + 2 * q4) * 144 + 8 * p4;
  auto wread = [&](int ks) {
#pragma unroll
    for (int t = 0; t < 5; ++t) wf[ks][t] = cat8(lds_tr(wwa + ks * 32 * 144 + 32 * t), lds_tr(wwa + ks * 32 * 144 + 32 * t + 144));
  };
#pragma unroll
  for (int ks = 0; ks < 5; ++ks) wread(ks);
  if (w < 8) STAMP_AT(5);
  if (w == 11) {
    lds_wait<4>(misc + M_OCNT, 8);
    const u64 t0 = __builtin_amdgcn_s_memrealtime();
    while (!__all(__hip_atomic_load((GAS unsigned*)(ws + WS_OF) + (rb * 8 + (lane & 7)) * 16, RLX_AGENT) == tag) && !spin_expired(t0)) {}
    __builtin_amdgcn_fence(__ATOMIC_ACQUIRE, "wavefront");
    lds_st(misc + M_GO2, 1u);
  } else { if (w >= 8) lds_wait<8>(misc + M_OCNT, 8); lds_wait<2>(misc + M_GO2, 1); }
  if (w < 8) STAMP_AT(6);
  f32x4 acc[5] = {};
  {
    u32x4 of[6];
#pragma unroll
    for (int ks = 0; ks < 6; ++ks) of[ks] = __builtin_amdgcn_raw_buffer_load_b128(wsr, WS_O2 + (((rb * 4 + rt) * 18 + 6 * kt + ks) * 64 + lane) * 16, 0, 16);
#pragma unroll
    for (int ks = 0; ks < 6; ++ks) {
#pragma unroll
      for (int t = 0; t < 5; ++t) acc[t] = MFMA16(wf[ks][t], PB(of[ks]), acc[t]);
      if (ks == 0) { SBAR(); wread(5); SBAR(); }
    }
  }
  if (w < 8) STAMP_AT(9);
  if (kt > 0) {
#pragma unroll
    for (int t = 0; t < 5; ++t) *(LAS f32x4*)(lds + L_PART + (((kt - 1) * 4 + rt) * 5 + t) * 1024 + lane * 16) = acc[t];
  }
  bar();
  if (w < 8) STAMP_AT(10);
  if (kt == 0) {
#pragma unroll
    for (int t = 0; t < 5; ++t) {
      const f32x4 s = acc[t] + *(const LAS f32x4*)(lds + L_PART + (rt * 5 + t) * 1024 + lane * 16) + *(const LAS f32x4*)(lds + L_PART + ((4 + rt) * 5 + t) * 1024 + lane * 16);
      if (t < 4 || g < 2) *(u32x2*)(out + (size_t)(rb * 64 + rt * 16 + r16) * (2 * ROWB) + ROWB + hg * 144 + t * 32 + g * 8) = u32x2{pk2(s[0], s[1]), pk2(s[2], s[3])};
    }
  } else if (b == 0 && w == 11) {
    const u64 t0 = __builtin_amdgcn_s_memrealtime();
    for (;;) {
      bool ok = true;
#pragma unroll
      for (int k = 0; k < 4; ++k) ok &= __hip_atomic_load((GAS unsigned*)(ws + WS_OF) + (lane + 64 * k) * 16, RLX_AGENT) == tag;
      if (__all(ok) || spin_expired(t0)) break;
    }
    if (lane == 0) __hip_atomic_store((GAS unsigned*)(ws + WS_EPOCH), tag, RLX_AGENT);
  }
  STAMP_FLUSH(0);
}

extern "C" void kernel_launch(void* const* d_in, const int* in_sizes, int n_in, void* d_out, int out_size, void* d_ws, size_t ws_size, hipStream_t stream) {
  mega_kernel<<<256, 768, 0, stream>>>((const char*)d_in[0], (const char*)d_in[1], (const char*)d_in[2], (const char*)d_in[3], (const char*)d_in[4], (const char*)d_in[5],
      (const char*)d_in[6], (const char*)d_in[7], (const char*)d_in[8], (const char*)d_in[9], (const char*)d_in[10], (const char*)d_in[11], (const char*)d_in[12],
      (const char*)d_in[13], (char*)d_out, (char*)d_ws);
}
```

```cpp
#include <hip/hip_runtime.h>
#include <hip/hip_bf16.h>
#include <type_traits>

typedef unsigned short u16;
typedef unsigned long long u64;
typedef __attribute__((ext_vector_type(8))) __bf16 bf16x8;
typedef __attribute__((ext_vector_type(2))) __bf16 bf16x2;
typedef __attribute__((ext_vector_type(8))) _Float16 f16x8;
typedef __attribute__((ext_vector_type(2))) _Float16 f16x2;
typedef __attribute__((ext_vector_type(4))) short s16x4;
typedef __attribute__((ext_vector_type(8))) short s16x8;
typedef __attribute__((ext_vector_type(4))) float f32x4;
typedef __attribute__((ext_vector_type(2))) float f32x2;
typedef __attribute__((ext_vector_type(16))) float f32x16;
typedef __attribute__((ext_vector_type(32))) float f32x32;
typedef __attribute__((ext_vector_type(4))) unsigned u32x4;
typedef __attribute__((ext_vector_type(2))) unsigned u32x2;
#define LAS __attribute__((address_space(3)))
#define GAS __attribute__((address_space(1)))
typedef LAS char* lptr;
#define DI __device__ __forceinline__
#define SBAR() __builtin_amdgcn_sched_barrier(0)
#define PIN(x) asm volatile("" : "+v"(x))
#define RLX_AGENT __ATOMIC_RELAXED, __HIP_MEMORY_SCOPE_AGENT

constexpr int ROWB = 1152;
constexpr int KV_V = 1216, KV_REC = 2496, KV_CHUNK = 20480;
constexpr unsigned WS_EPOCH = 0, WS_KVF = 256, WS_UF = 8704, WS_OF = 16384, WS_YF = 32768, WS_KV = 40960, WS_O2 = WS_KV + 8 * KV_CHUNK;
constexpr unsigned WS_UT = WS_O2 + 2048 * ROWB, WS_YT = WS_UT + 2048 * ROWB, WS_END = WS_YT + 2048 * ROWB;
constexpr int L_KV = 0, L_KC = 40960, KC_COPY = 4160, KC_CH = 20736, L_QX = L_KC + 4 * KC_COPY, L_K0 = 123904;
constexpr int L_TAB = 128512, TAB_W = 8704, L_U = 132608, U_CH = 7680, L_WO = L_KC, L_OX = L_U, L_PART = 0, L_MISC = 163328;
constexpr int M_TAG = 0, M_KMAX = 4, M_GO = 8, M_GO2 = 12, M_CNT = 16, M_N2 = 20, M_OCNT = 24, M_WOR = 48, M_DONE = 64, M_RDY = 96, M_OXR = 224, M_QL = 128, M_QF = 144, M_HS = 160, M_R0 = 192;
constexpr int QS = 17408, L_QR = 23552, L_KW = L_QR + 2 * QS, KW_A = 27648, L_KX = L_KW + 2 * KW_A;
constexpr int CODE_LINES = 296;
constexpr unsigned EP_STEP = 0x9E3779B9u;
constexpr float LOG2E = 1.4426950408889634f;
constexpr float QSCALE = 0.23570226039551584f * LOG2E;

#define STAMP_DECL
#define STAMP_AT(idx)
#define STAMP_FLUSH(kid)

DI unsigned pk2(float lo, float hi) { f32x2 v = {lo, hi}; bf16x2 b = __builtin_convertvector(v, bf16x2); return __builtin_bit_cast(unsigned, b); }
DI unsigned pkh(f32x2 v) { f16x2 b = __builtin_convertvector(v, f16x2); return __builtin_bit_cast(unsigned, b); }
DI float bflo(unsigned v) { return __uint_as_float(v << 16); }
DI float bfhi(unsigned v) { return __uint_as_float(v & 0xffff0000u); }
DI float sq2(unsigned v) { return bflo(v) * bflo(v) + bfhi(v) * bfhi(v); }

DI void glds16(const void* sbase, unsigned voff, unsigned lds_dst) {
  unsigned sv;
  asm volatile("s_mov_b32 %0, m0\n\ts_mov_b32 m0, %3\n\ts_nop 0\n\tglobal_load_lds_dwordx4 %1, %2\n\ts_mov_b32 m0, %0"
               : "=&s"(sv) : "v"(voff), "s"(sbase), "s"(lds_dst) : "memory");
}
DI void glds16_8lanes(const void* sbase, unsigned voff, unsigned lds_dst) {
  unsigned sv;
  asm volatile("s_mov_b32 %0, m0\n\ts_mov_b32 m0, %3\n\ts_mov_b64 exec, 0xff\n\tglobal_load_lds_dwordx4 %1, %2\n\ts_mov_b64 exec, -1\n\ts_mov_b32 m0, %0"
               : "=&s"(sv) : "v"(voff), "s"(sbase), "s"(lds_dst) : "memory");
}
template <int N> DI void wait_vm_bar() { asm volatile("s_waitcnt vmcnt(%0) lgkmcnt(0)\n\ts_barrier" ::"i"(N) : "memory"); }
DI void bar() { asm volatile("s_waitcnt lgkmcnt(0)\n\ts_barrier" ::: "memory"); }
DI void bar_raw() { asm volatile("s_barrier" ::: "memory"); }
DI bf16x8 lds_b128(lptr p) { return *(const LAS bf16x8*)p; }
DI u32x4 lds_u128(lptr p) { return *(const LAS u32x4*)p; }
DI s16x4 lds_tr(lptr p) { return __builtin_amdgcn_ds_read_tr16_b64_v4i16((LAS s16x4*)p); }
DI bf16x8 cat8(s16x4 a, s16x4 b) { s16x8 v = __builtin_shufflevector(a, b, 0, 1, 2, 3, 4, 5, 6, 7); return __builtin_bit_cast(bf16x8, v); }
#define MFMA16(a, b, c) __builtin_amdgcn_mfma_f32_16x16x32_bf16(a, b, c, 0, 0, 0)
#define MFMA32(a, b, c) __builtin_amdgcn_mfma_f32_32x32x16_bf16(a, b, c, 0, 0, 0)
#define MFMA16H(a, b, c) __builtin_amdgcn_mfma_f32_16x16x32_f16(a, b, c, 0, 0, 0)
#define MFMA32K8(a, b, c) __builtin_amdgcn_mfma_f32_32x32x8bf16_1k(a, b, c, 0, 0, 0)
#define EX(v) __builtin_amdgcn_exp2f(v)
#define PB(P) __builtin_bit_cast(bf16x8, P)

template <int CTRL> DI float dpp_mov(float v) { return __uint_as_float(__builtin_amdgcn_update_dpp(0u, __float_as_uint(v), CTRL, 0xf, 0xf, true)); }
DI float row_max(float v) {
  v = fmaxf(v, dpp_mov<0xB1>(v)); v = fmaxf(v, dpp_mov<0x4E>(v)); v = fmaxf(v, dpp_mov<0x141>(v)); v = fmaxf(v, dpp_mov<0x140>(v));
  return v;
}
DI float wave_max(float v) {
  v = row_max(v);
  auto r1 = __builtin_amdgcn_permlane16_swap(__float_as_uint(v), __float_as_uint(v), false, false);
  v = fmaxf(__uint_as_float(r1[0]), __uint_as_float(r1[1]));
  auto r2 = __builtin_amdgcn_permlane32_swap(__float_as_uint(v), __float_as_uint(v), false, false);
  return fmaxf(__uint_as_float(r2[0]), __uint_as_float(r2[1]));
}
DI float sum_rows(float v) {
  auto r1 = __builtin_amdgcn_permlane16_swap(__float_as_uint(v), __float_as_uint(v), false, false);
  v = __uint_as_float(r1[0]) + __uint_as_float(r1[1]);
  auto r2 = __builtin_amdgcn_permlane32_swap(__float_as_uint(v), __float_as_uint(v), false, false);
  return __uint_as_float(r2[0]) + __uint_as_float(r2[1]);
}
DI float other_half(float v) { auto rr = __builtin_amdgcn_permlane32_swap(__float_as_uint(v), __float_as_uint(v), false, false); return (threadIdx.x & 32) ? __uint_as_float(rr[0]) : __uint_as_float(rr[1]); }
DI bool spin_expired(u64 t0) { __builtin_amdgcn_s_sleep(4); return __builtin_amdgcn_s_memrealtime() - t0 > 400000ull; }
struct CM { f32x2 e, r; };
DI CM mk(f32x2 e) { return CM{e, f32x2{-e[1], e[0]}}; }
DI f32x2 cmul(f32x2 a, const CM& m) { return __builtin_elementwise_fma(f32x2{a[1], a[1]}, m.r, f32x2{a[0], a[0]} * m.e); }
DI f32x2 cexp2(float lg, float rev) { rev -= __builtin_rintf(rev); const float e = EX(lg); return f32x2{e * __builtin_amdgcn_cosf(rev), e * __builtin_amdgcn_sinf(rev)}; }

#define SPIN_SLEEP 8
DI unsigned lds_ld(lptr p) { return *(const volatile LAS unsigned*)p; }
DI void lds_st(lptr p, unsigned v) { *(volatile LAS unsigned*)p = v; }
DI void cfence() { asm volatile("" ::: "memory"); }
template <int SL = SPIN_SLEEP> DI void lds_wait(lptr p, unsigned need) { while (!__all(lds_ld(p) >= need)) __builtin_amdgcn_s_sleep(SL); cfence(); }
DI int rsw(int r) { return (r & ~7) | ((r & 1) << 2) | ((r >> 1) & 3); }
DI void tr8(const u32x4 (&r)[8], u32x4 (&o)[8]) {
#pragma unroll
  for (int k = 0; k < 8; ++k)
#pragma unroll
    for (int m = 0; m < 4; ++m) o[k][m] = __builtin_amdgcn_perm(r[2 * m + 1][k >> 1], r[2 * m][k >> 1], (k & 1) ? 0x07060302u : 0x05040100u);
}
#define EXP 0
#define TU_AFTER 10
#define QSL 0
#define YAUX 16
#define OAUX 0
#define VZERO 1
#define USE2B 0
#define CLOW 2
#define NB 2
#define RSL 2
#define CN -1
#define CN2 -1
#define QDELAY 0
#define TUW 0
#define POLL1 0
#define PSL 0
#define TYN 13
#define RSPLIT 6
#define HOLD 1
#define SYNC1 0
#define TUHOLD 1
#define TUS 0
#define CONV_S3 0
#define CONV_SLEEP 0
#define HPRIO 2

DI void kv_half(lptr lds, int sel, int tq, int k0, f32x4& aM, f32x4& aR) {
  const int lane = threadIdx.x & 63, r16 = lane & 15, g = lane >> 4, q4 = (lane & 15) >> 2, p4 = lane & 3;
  const lptr ma_ = lds + L_KW + sel * KW_A + (8 * g + 2 * q4) * 48 + k0 * 1536, ra_ = ma_ + 40, mb_ = ma_ + 8 * p4;
  lptr xa[2];
#pragma unroll
  for (int ks = 0; ks < 2; ++ks) xa[ks] = lds + L_KX + ((k0 + ks) >> 1) * 4096 + (16 * tq + r16) * 128 + (((4 * ((k0 + ks) & 1) + g) ^ ((r16 >> 1) & 7)) * 16);
  bf16x8 xf[3], fm[3], fr[3];
  auto rd = [&](int i) { xf[i % 3] = lds_b128(xa[i & 1] + (i >> 1) * 4096); fm[i % 3] = cat8(lds_tr(mb_ + i * 1536), lds_tr(mb_ + i * 1536 + 48)); fr[i % 3] = cat8(lds_tr(ra_ + i * 1536), lds_tr(ra_ + i * 1536 + 48)); };
  rd(0); rd(1);
  if (sel == 0) {
#pragma unroll
    for (int i = 0; i < 9; ++i) { if (i + 2 < 9) rd(i + 2); SBAR(); aM = MFMA16(fm[i % 3], xf[i % 3], aM); aR = MFMA16(fr[i % 3], xf[i % 3], aR); SBAR(); }
  } else {
#pragma unroll
    for (int i = 0; i < 9; ++i) { if (i + 2 < 9) rd(i + 2); SBAR(); aM = MFMA16(xf[i % 3], fm[i % 3], aM); aR = MFMA16(xf[i % 3], fr[i % 3], aR); SBAR(); }
  }
}

struct Args { const char *u, *Are, *Aim, *Bre, *Bim, *Cre, *Cim, *ldt, *Dsk, *x, *wq, *wk, *wv, *wo; char *out, *ws; };
typedef const __attribute__((address_space(4))) Args* ArgP;
#define ARGS() ArgP A_ = (ArgP)__builtin_amdgcn_kernarg_segment_ptr(); asm volatile("" : "+s"(A_)); char* const ws = A_->ws; \
  const __amdgpu_buffer_rsrc_t wsr = __builtin_amdgcn_make_buffer_rsrc(ws, 0, WS_END, 0x00020000)
#define OUTR() __builtin_amdgcn_make_buffer_rsrc(A_->out, 0, 2048 * 2 * ROWB, 0x00020000)

__global__ __launch_bounds__(768) void mega_kernel(Args) {
  __shared__ __attribute__((aligned(16))) char smem[163840];
  const lptr lds = (lptr)smem, misc = lds + L_MISC;
  const unsigned lds0 = (unsigned)(size_t)lds;
  const int tid = threadIdx.x, lane = tid & 63, w = __builtin_amdgcn_readfirstlane(tid >> 6);
  const int b = blockIdx.x, xcd = b & 7, j = b >> 3, rb = xcd * 4 + (j & 3), hg = j >> 2;
  const int c = lane & 31, h = lane >> 5, r16 = lane & 15, g = lane >> 4, q4 = (lane & 15) >> 2, p4 = lane & 3;
  const f32x16 zero = {};
  const bool kvp = j >= 24, ssm = !kvp;
  STAMP_DECL; STAMP_AT(0);
#define PROBE_AT(n)
  unsigned pf0, pf1;
  asm volatile("s_load_dword %0, %2, 0x0\n\ts_load_dword %1, %2, 0x40" : "=&s"(pf0), "=&s"(pf1) : "s"(__builtin_amdgcn_kernarg_segment_ptr()));
  unsigned pfc = 0;
  if (w < 4) {
    const int ln = (j * 4 + w) * 3 + (lane < 2 ? lane : 2);
    u64 pc;
    asm volatile("s_getpc_b64 %1\n\tglobal_load_dword %0, %2, %1" : "=&v"(pfc), "=&s"(pc) : "v"((ln < CODE_LINES ? ln : 0) * 128));
  }
#define PF_DONE() asm volatile("s_waitcnt lgkmcnt(0)" :: "s"(pf0), "s"(pf1))

  auto tu_task = [&](int task) {
    ARGS();
    const unsigned e0 = __hip_atomic_load((GAS unsigned*)(ws + WS_EPOCH), RLX_AGENT);
    const char* src = A_->u + (size_t)((task & 31) * 64 + (lane >> 3) * 8) * ROWB + (task >> 5) * 128 + (lane & 7) * 16;
    u32x4 ra[8], o[8];
#pragma unroll
    for (int e = 0; e < 8; ++e) ra[e] = *(const u32x4*)(src + e * ROWB);
    SBAR(); PF_DONE(); bar_raw(); SBAR();
    tr8(ra, o);
    if (TUHOLD == 1 && w == 11) { PIN(o[0]); PIN(o[7]); cfence(); lds_st(misc + M_CNT, 5u); }
#pragma unroll
    for (int e = 0; e < 8; ++e) __builtin_amdgcn_raw_buffer_store_b128(o[e], wsr, WS_UT + ((task >> 5) * 64 + (lane & 7) * 8 + e) * 4096 + (task & 31) * 128 + (lane >> 3) * 16, 0, 16);
    if (TUHOLD == 2 && w == 11) { cfence(); lds_st(misc + M_CNT, 5u); }
    asm volatile("s_waitcnt vmcnt(0)" ::: "memory");
    if (lane == 0) __hip_atomic_store((GAS unsigned*)(ws + WS_UF) + task, e0 + EP_STEP, RLX_AGENT);
    return e0;
  };

  if (w < 8) {
    const int hd = w & 3, kh = w >> 2;
    if (ssm) __builtin_amdgcn_s_setprio(3);
    if (w == 0) lds_st(misc + lane * 4, 0u);

    u32x4 qau, qbu; u32x2 qa, qb; unsigned q2lo, nm; bool fast, prepped = false;
    auto qprep = [&]() {
      qau = lds_u128(lds + L_QX + hd * KC_CH + (32 * kh + c) * 48 + 16 * h);
      qbu = lds_u128(lds + L_QX + hd * KC_CH + (32 * (kh ^ 1) + c) * 48 + 16 * h);
      q2lo = *(const LAS unsigned*)(lds + L_QX + hd * KC_CH + ((lane + 32 * kh) & 63) * 48 + 32);
      const float pa = sq2(qau[0]) + sq2(qau[1]) + sq2(qau[2]) + sq2(qau[3]), pb = sq2(qbu[0]) + sq2(qbu[1]) + sq2(qbu[2]) + sq2(qbu[3]);
      auto rr = __builtin_amdgcn_permlane32_swap(__float_as_uint(pa), __float_as_uint(pb), false, false);
      float n2 = __uint_as_float(rr[0]) + __uint_as_float(rr[1]) + sq2(q2lo);
      PIN(n2);
      lds_wait<1>(misc + M_GO, 1);
      const float bound = __builtin_amdgcn_sqrtf(n2 * *(const LAS float*)(misc + M_KMAX)) * 1.01f;
      fast = __all(bound <= 40.f);
      nm = fast ? pk2(-bound, 0.f) : 0u;
      qa = h ? u32x2{0u, 0u} : u32x2{q2lo, nm}; qb = h ? u32x2{q2lo, nm} : u32x2{0u, 0u};
    };
    if (w >= 4) {
      ARGS();
      unsigned ep7 = 0;
      if (w == 7) ep7 = __hip_atomic_load((GAS unsigned*)(ws + WS_EPOCH), RLX_AGENT);
      const int iw = w - 4, rot = (2 * (hg + (j & 3))) % 9;
      unsigned xo[2], wo_[3];
#pragma unroll
      for (int i = 0; i < 2; ++i) { const int xrow = 16 * iw + 8 * i + (lane >> 3); xo[i] = xrow * ROWB + (((lane & 7) ^ ((xrow >> 1) & 7)) * 16); }
#pragma unroll
      for (int i = 0; i < 3; ++i) { const int cc = (i < 2 ? 128 * iw + 64 * i : 512) + lane; wo_[i] = rsw(cc / 9) * ROWB + (cc % 9) * 16; }
      const char* xb = A_->x + (size_t)rb * 64 * ROWB;
      const char* wb = A_->wq + hg * 144;
      const lptr qlw = misc + M_QL + iw * 4;
      PF_DONE(); bar();
      if (QDELAY && !kvp) __builtin_amdgcn_s_sleep(QDELAY);
      int st = rot, slot = 0;
#pragma unroll 1
      for (int s = 0; s < 9; ++s) {
        if (TUHOLD && s == TUS && ssm) lds_wait<2>(misc + M_CNT, 5);
        if (s == 2 && kvp) {
          asm volatile("s_waitcnt vmcnt(0)" ::: "memory"); lds_st(qlw, 2u);
          if (HOLD) lds_wait<2>(misc + M_CNT, 5); else lds_wait<2>(misc + M_HS + p4 * 4, 2);
        }
        if (s >= 5) lds_wait<1>(misc + M_QF + p4 * 4, s - 4);
        const unsigned dst = lds0 + L_QR + slot * QS;
        const char* xs = xb + st * 128; const char* wsrc = wb + (size_t)st * 64 * ROWB;
        glds16(xs, xo[0], dst + iw * 2048); glds16(xs, xo[1], dst + iw * 2048 + 1024);
        glds16(wsrc, wo_[0], dst + 8192 + iw * 2048); glds16(wsrc, wo_[1], dst + 8192 + iw * 2048 + 1024);
        if (iw == 0) { glds16(wsrc, wo_[2], dst + 16384); asm volatile("s_waitcnt vmcnt(10)" ::: "memory"); }
        else asm volatile("s_waitcnt vmcnt(8)" ::: "memory");
        if (ssm || s > 2) lds_st(qlw, s > 0 ? s - 1 : 0);
        st = st == 8 ? 0 : st + 1; slot = slot == 4 ? 0 : slot + 1;
      }
      STAMP_AT(1);
      if (w == 4) asm volatile("s_waitcnt vmcnt(5)" ::: "memory"); else asm volatile("s_waitcnt vmcnt(4)" ::: "memory");
      lds_st(qlw, 8u);
      unsigned tag;
      asm volatile("s_waitcnt vmcnt(0)\n\tv_add_u32 %0, %2, %1" : "=v"(tag) : "v"(ep7), "s"(EP_STEP) : "memory");
      lds_st(qlw, 9u);
      if (ssm) {
      const u64 t0 = __builtin_amdgcn_s_memrealtime();
      if (w == 7) {
        u64 fv;
        do fv = __hip_atomic_load((GAS u64*)(ws + WS_KVF + lane * 128), RLX_AGENT); while (!__all((unsigned)(fv >> 32) == tag) && !spin_expired(t0));
        __builtin_amdgcn_fence(__ATOMIC_ACQUIRE, "wavefront");
        const float km = wave_max(__uint_as_float((unsigned)fv));
        if (lane == 0) *(LAS u32x2*)(misc + M_TAG) = u32x2{tag, __float_as_uint(km)};
        cfence(); lds_st(misc + M_GO, 1u);
      } else lds_wait<1>(misc + M_GO, 1);
      STAMP_AT(12);
      const int il = tid - 256, kc = il < 160 ? il : 159;
      u32x4 c0[10];
      const u32x4 k0 = __builtin_amdgcn_raw_buffer_load_b128(wsr, WS_KV + 7 * KV_CHUNK + (6 + (kc >= 80)) * KV_REC + KV_V + (kc >= 80 ? kc - 80 : kc) * 16, 0, 16);
#pragma unroll
      for (int i = 0; i < 10; ++i) c0[i] = __builtin_amdgcn_raw_buffer_load_b128(wsr, WS_KV + il * 16 + i * 4096, 0, 16);
      SBAR();
      if (__all(lds_ld(misc + M_QF + p4 * 4) >= 10u)) { cfence(); qprep(); prepped = true; }
      SBAR();
      *(LAS u32x4*)(lds + L_K0 + kc * 16) = k0;
#pragma unroll
      for (int i = 0; i < 5; ++i) *(LAS u32x4*)(lds + L_KV + il * 16 + i * 4096) = c0[i];
      cfence(); lds_st(misc + M_R0 + iw * 4, 1u);
      if (!prepped) lds_wait<1>(misc + M_QF + p4 * 4, 6);
#pragma unroll
      for (int i = 5; i < 10; ++i) *(LAS u32x4*)(lds + L_KV + il * 16 + i * 4096) = c0[i];
      cfence(); lds_st(misc + M_RDY + iw * 4, 4u);
      }
      STAMP_AT(2);
    } else {
      lptr xa[2];
#pragma unroll
      for (int ks = 0; ks < 2; ++ks) xa[ks] = lds + L_QR + (16 * w + r16) * 128 + (((4 * ks + g) ^ ((r16 >> 1) & 7)) * 16);
      const lptr wa = lds + L_QR + 8192 + (8 * g + 2 * q4) * 144 + 8 * p4;
      const lptr qlr = misc + M_QL + p4 * 4, qfw = misc + M_QF + w * 4;
      f32x4 acc[5] = {};
      if (kvp) {
        ARGS();
        const int xrow = 8 * w + (lane >> 3);
        const unsigned xo = xrow * ROWB + (((lane & 7) ^ ((xrow >> 1) & 7)) * 16);
        const char* xb = A_->x + (size_t)(xcd * 8 + (j - 24)) * 32 * ROWB;
#pragma unroll
        for (int i = 0; i < 9; ++i) glds16(xb + i * 128, xo, lds0 + L_KX + i * 4096 + w * 1024);
      }
      if (j < 12 && w == 3) tu_task(192 + xcd * 12 + j);
      else { PF_DONE(); bar(); }
      if (kvp) {
        asm volatile("s_waitcnt vmcnt(0)" ::: "memory"); lds_st(misc + M_HS + 16 + w * 4, 1u);
        lds_wait<1>(misc + M_HS + (lane & 7) * 4, 1);
        f32x4 aM = {0.f, 0.f, 0.f, 0.f}, aR = aM;
        kv_half(lds, w >> 1, w & 1, 9, aM, aR);
        *(LAS f32x4*)(lds + w * 2048 + lane * 16) = aM; *(LAS f32x4*)(lds + w * 2048 + 1024 + lane * 16) = aR;
        cfence(); lds_st(misc + M_HS + 16 + w * 4, 2u);
      }
      lptr qw[10];
#pragma unroll
      for (int i = 0; i < 10; ++i) { const int n = 16 * (i >> 1) + 4 * g + 2 * (i & 1), hh = n / 18; qw[i] = n < 72 ? lds + L_QX + hh * KC_CH + (16 * w + r16) * 48 + (n - 18 * hh) * 2 : lds + L_K0 + 2560 + lane * 4; PIN(qw[i]); }
      bf16x8 xf[2][2], wf[2][2][5];
      asm volatile("s_waitcnt vmcnt(0)" :: "v"(pfc));
      lds_wait<1>(qlr, 1);
      STAMP_AT(1); if (ssm) PROBE_AT(3);
#pragma unroll
      for (int s = 0; s < 10; ++s) {
        const int so = (s % 5) * QS, bf = s & 1;
        unsigned ql = 0;
#pragma unroll
        for (int i = 0; i < 10; ++i) {
          const int ks = i / 5, t = i % 5;
          if (s < 9) {
            if (i == 0 && s < 8) ql = lds_ld(qlr);
            if (t == 0) xf[bf][ks] = lds_b128(xa[ks] + so);
            wf[bf][ks][t] = cat8(lds_tr(wa + so + ks * 32 * 144 + 32 * t), lds_tr(wa + so + ks * 32 * 144 + 32 * t + 144));
            if (i == 9) { cfence(); lds_st(qfw, s + 1); }
          }
          if (s > 0) { acc[t] = MFMA16(wf[bf ^ 1][ks][t], xf[bf ^ 1][ks], acc[t]); PIN(acc[t]); }
          SBAR();
        }
        if (s < 8 && !__all(ql >= (unsigned)(s + 2))) lds_wait<1>(qlr, s + 2);
        cfence(); SBAR();
      }
      lds_wait<0>(misc + M_QF + p4 * 4, 9);
#pragma unroll
      for (int t = 0; t < 5; ++t)
#pragma unroll
        for (int e = 0; e < 2; ++e) {
          *(LAS unsigned*)qw[2 * t + e] = pk2(acc[t][2 * e] * QSCALE, acc[t][2 * e + 1] * QSCALE);
        }
      cfence(); lds_st(qfw, 10u);
      STAMP_AT(2); if (ssm) PROBE_AT(4);
    }
    if (!prepped) { lds_wait<1>(misc + M_QF + p4 * 4, 10); qprep(); }
    const bf16x8 q1a = PB(qau), q1b = PB(qbu);
    lptr vb = lds + L_KV + kh * KV_REC + h * 304 + (c < 18 ? c * 16 : (c == 18 || c == 22) ? 18 * 16 : 0x40000);
    lptr kb = lds + L_KV + kh * KV_REC + KV_V + lane * 16;
    lptr k2b = lds + L_KV + kh * KV_REC + KV_V + 1024 + c * 8;
    const lptr syncr = misc + M_DONE + r16 * 4, donep = misc + M_DONE + w * 4;
    auto ring_ok = [&](unsigned v, unsigned need) { return ((unsigned)__builtin_amdgcn_ballot_w64(v >= need) & 0xf00u) == 0xf00u; };
    auto ring_wait = [&](unsigned need) { while (!ring_ok(lds_ld(syncr), need)) __builtin_amdgcn_s_sleep(RSL); cfence(); };
    f32x16 Oa = zero, Ob = zero;
    PIN(qa); PIN(qb); PIN(Oa); PIN(Ob);
    const s16x4 q2a = __builtin_bit_cast(s16x4, qa), q2b = __builtin_bit_cast(s16x4, qb);
    lds_wait<1>(misc + M_R0 + p4 * 4, 1);
    __builtin_amdgcn_s_setprio(1);
    STAMP_AT(3);
    s16x4 kf2 = *(const LAS s16x4*)(lds + L_K0 + kh * 1280 + 1024 + c * 8);
    bf16x8 kf = lds_b128(lds + L_K0 + kh * 1280 + lane * 16);
    cfence(); lds_st(donep, 1u);
    float ma = 0.f, mb = 0.f;
    if (fast) {
      f32x16 Aa, Ab, Ba, Bb;
      Aa = MFMA32K8(kf2, q2a, zero); Aa = MFMA32(kf, q1a, Aa);
      Ab = MFMA32K8(kf2, q2b, zero); Ab = MFMA32(kf, q1b, Ab);
      SBAR(); kf2 = *(const LAS s16x4*)k2b; SBAR(); bf16x8 vf0 = lds_b128(vb); SBAR(); kf = lds_b128(kb); SBAR(); bf16x8 vf1 = lds_b128(vb + 608); SBAR();
      u32x4 p0, p1;
#define GRP(MF, RD, S, i, P, jj) do { float e0 = EX(S[i]), e1 = EX(S[i + 1]); SBAR(); MF; RD; SBAR(); float e2 = EX(S[i + 2]), e3 = EX(S[i + 3]); P[jj] = pk2(e0, e1); P[jj + 1] = pk2(e2, e3); PIN(P); SBAR(); } while (0)
#define HALF1(Ca, Cb, Na, Nb, RD) do { \
      GRP(Na = MFMA32K8(kf2, q2a, zero), , Ca, 4, p0, 2); \
      GRP(Oa = MFMA32(vf0, PB(p0), Oa), RD, Ca, 8, p1, 0); \
      GRP(Na = MFMA32(kf, q1a, Na), , Ca, 12, p1, 2); \
      GRP(Oa = MFMA32(vf1, PB(p1), Oa), , Cb, 0, p0, 0); } while (0)
#define HALF2(Cb, Na, Nb, off) do { \
      GRP(Nb = MFMA32K8(kf2, q2b, zero), kf2 = *(const LAS s16x4*)(k2b + (off)), Cb, 4, p0, 2); \
      GRP(Ob = MFMA32(vf0, PB(p0), Ob), vf0 = lds_b128(vb + (off)), Cb, 8, p1, 0); \
      GRP(Nb = MFMA32(kf, q1b, Nb), kf = lds_b128(kb + (off)), Cb, 12, p1, 2); \
      GRP(Ob = MFMA32(vf1, PB(p1), Ob), vf1 = lds_b128(vb + (off) + 608), Na, 0, p0, 0); } while (0)
      const f32x32 zero32 = {};
      const u32x2 q2u = {q2lo, nm};
      const s16x4 q2 = __builtin_bit_cast(s16x4, q2u);
      p0[0] = pk2(EX(Aa[0]), EX(Aa[1])); p0[1] = pk2(EX(Aa[2]), EX(Aa[3])); p0[2] = 0; p0[3] = 0; p1 = p0;
      STAMP_AT(11); PROBE_AT(1);
#define RWAIT(val, need) do { if (__builtin_expect(!ring_ok(val, need), 0)) ring_wait(need); } while (0)
#define BALANCE(val, mine) asm volatile("s_cmp_lt_u32 %0, %1\n\ts_setprio 1\n\ts_cbranch_scc0 1f\n\ts_setprio 0\n1:" :: "s"((unsigned)__builtin_amdgcn_readlane(val, w ^ 4)), "s"(mine) : "scc")
      for (unsigned p = 0; p < 8; ++p) {
        unsigned sa;
        HALF1(Aa, Ab, Ba, Bb, ); HALF2(Ab, Ba, Bb, 2 * KV_REC);
        cfence(); lds_st(donep, 2 * p + 2); SBAR();
        HALF1(Ba, Bb, Aa, Ab, sa = lds_ld(syncr));
        BALANCE(sa, 2 * p + 2);
        RWAIT(sa, 2 * p + 2);
        cfence(); SBAR();
        HALF2(Bb, Aa, Ab, 4 * KV_REC);
        HALF1(Aa, Ab, Ba, Bb, ); HALF2(Ab, Ba, Bb, 6 * KV_REC);
        cfence(); lds_st(donep, 2 * p + 3); SBAR();
        HALF1(Ba, Bb, Aa, Ab, sa = lds_ld(syncr));
        BALANCE(sa, 2 * p + 3);
        const int adv = (p & 1) ? -KV_CHUNK : KV_CHUNK;
        vb += adv; kb += adv; k2b += adv;
        RWAIT(sa, 2 * p + 3 + SYNC1);
        cfence(); SBAR();
        HALF2(Bb, Aa, Ab, 0);
      }
    } else {
      ma = -INFINITY; mb = -INFINITY;
      for (int i = 0; i < 32; ++i) {
        if (i && (i & 1) == 0) ring_wait((i >> 1) + 1);
        const int off = ((i >> 2) & 1) * KV_CHUNK + (i & 3) * 2 * KV_REC;
        f32x16 Sa = MFMA32K8(kf2, q2a, zero), Sb = MFMA32K8(kf2, q2b, zero);
        Sa = MFMA32(kf, q1a, Sa); Sb = MFMA32(kf, q1b, Sb);
        kf = lds_b128(kb + off); kf2 = *(const LAS s16x4*)(k2b + off);
        const bf16x8 vf0 = lds_b128(vb + off), vf1 = lds_b128(vb + off + 608);
        if (i & 1) { cfence(); lds_st(donep, (i >> 1) + 2); }
        float ta = ma, tb = mb;
#pragma unroll
        for (int r = 0; r < 16; ++r) { ta = fmaxf(ta, Sa[r]); tb = fmaxf(tb, Sb[r]); }
        ta = fmaxf(ta, other_half(ta)); tb = fmaxf(tb, other_half(tb));
        const float fa = EX(ma - ta), fb = EX(mb - tb);
        ma = ta; mb = tb;
        u32x4 pp[4];
#pragma unroll
        for (int r = 0; r < 8; ++r) {
          Oa[2 * r] *= fa; Oa[2 * r + 1] *= fa; Ob[2 * r] *= fb; Ob[2 * r + 1] *= fb;
          pp[r >> 2][r & 3] = pk2(EX(Sa[2 * r] - ma), EX(Sa[2 * r + 1] - ma));
          pp[2 + (r >> 2)][r & 3] = pk2(EX(Sb[2 * r] - mb), EX(Sb[2 * r + 1] - mb));
        }
        Oa = MFMA32(vf0, PB(pp[0]), Oa); Ob = MFMA32(vf0, PB(pp[2]), Ob);
        Oa = MFMA32(vf1, PB(pp[1]), Oa); Ob = MFMA32(vf1, PB(pp[3]), Ob);
      }
    }
    STAMP_AT(4); PROBE_AT(2);
    __builtin_amdgcn_s_setprio(0);
    lds_wait<2>(misc + M_WOR + p4 * 4, 1);
    {
      const lptr ox = lds + L_OX + w * 3072 + lane * 16;
      *(LAS f32x4*)(ox) = f32x4{Ob[0], Ob[1], Ob[2], Ob[3]};
      *(LAS f32x4*)(ox + 1024) = f32x4{Ob[4], Ob[5], Ob[6], Ob[7]};
      *(LAS f32x4*)(ox + 2048) = f32x4{Ob[8], Ob[9], Ob[10], mb};
      cfence(); lds_st(misc + M_OXR + w * 4, 1u);
    }
    lds_wait<2>(misc + M_OXR + (w ^ 4) * 4, 1);
    STAMP_AT(7);
    {
      ARGS();
      const lptr ox = lds + L_OX + (w ^ 4) * 3072 + lane * 16;
      const f32x4 t0 = *(const LAS f32x4*)(ox), t1 = *(const LAS f32x4*)(ox + 1024), t2 = *(const LAS f32x4*)(ox + 2048);
      const float mm = fmaxf(ma, t2[3]);
      float fa = EX(ma - mm), fo = EX(t2[3] - mm);
      const float inv = __builtin_amdgcn_rcpf(Oa[10] * fa + t2[2] * fo);
      fa *= inv; fo *= inv;
      const int tok = 32 * kh + c, gm = 9 * hg + 2 * hd + h, g8 = 9 * hg + 8;
      const unsigned ob = WS_O2 + ((rb * 4 + (tok >> 4)) * 72 * 16 + (tok & 15)) * 16;
      const u32x4 v = {pk2(Oa[0] * fa + t0[0] * fo, Oa[1] * fa + t0[1] * fo), pk2(Oa[2] * fa + t0[2] * fo, Oa[3] * fa + t0[3] * fo),
                       pk2(Oa[4] * fa + t1[0] * fo, Oa[5] * fa + t1[1] * fo), pk2(Oa[6] * fa + t1[2] * fo, Oa[7] * fa + t1[3] * fo)};
      __builtin_amdgcn_raw_buffer_store_b128(v, wsr, ob + gm * 256, 0, 16);
      if (h == 0) __builtin_amdgcn_raw_buffer_store_b32(pk2(Oa[8] * fa + t2[0] * fo, Oa[9] * fa + t2[1] * fo), wsr, ob + g8 * 256 + hd * 4, 0, 16);
      const unsigned tg = *(const LAS unsigned*)(misc + M_TAG);
      asm volatile("s_waitcnt vmcnt(0)" ::: "memory");
      if (lane == 0) {
        if (__hip_atomic_fetch_add((LAS unsigned*)(misc + M_OCNT), 1u, __ATOMIC_RELAXED, __HIP_MEMORY_SCOPE_WORKGROUP) == 7u)
          __hip_atomic_store((GAS unsigned*)(ws + WS_OF) + (rb * 8 + hg) * 16, tg, RLX_AGENT);
      }
      STAMP_AT(8);
    }
  } else {
    const int hw = w - 8, hl = tid - 512;
    __builtin_amdgcn_s_setprio(HPRIO);
    unsigned ep;
#define LOAD_EP() ep = __hip_atomic_load((GAS unsigned*)(ws + WS_EPOCH), RLX_AGENT)
#define GET_TAG(t) unsigned t; asm volatile("v_add_u32 %0, %2, %1" : "=v"(t) : "v"(ep), "s"(EP_STEP))
    const int d = xcd * 72 + j * 3 + hw;
    const bool own = ssm && hw < 3;
    unsigned woff[11];
    auto wo_offsets = [&]() {
#pragma unroll
      for (int i = 0; i < 11; ++i) {
        const int cc = 64 * i + (i < 10 ? lane : lane & 7), row = cc / 9, kk = rsw(row), cl = kk >> 3, e = kk & 7;
        woff[i] = (cl < 8 ? 18 * (cl >> 1) + 4 * (cl & 1) + (e & 3) + 2 * (e & 4) : 18 * (e >> 1) + 16 + (e & 1)) * ROWB + (cc - 9 * row) * 16;
        PIN(woff[i]);
      }
    };
    unsigned dsk = 0;
    f32x4 kacc[2][2] = {}; float amax = 0.f; int ce = 0;
    u32x4 ut[4]; bool have_u = false; unsigned uf = 0;
    if (own) {
      ARGS();
      const unsigned po = d * 256 + lane * 4;
      const unsigned vdt = *(const u16*)(A_->ldt + d * 2);
      const unsigned vAr = *(const unsigned*)(A_->Are + po), vAi = *(const unsigned*)(A_->Aim + po), vBr = *(const unsigned*)(A_->Bre + po), vBi = *(const unsigned*)(A_->Bim + po);
      const unsigned vCr = *(const unsigned*)(A_->Cre + po), vCi = *(const unsigned*)(A_->Cim + po);
      dsk = *(const u16*)(A_->Dsk + d * 2);
      LOAD_EP();
      SBAR(); PF_DONE(); bar_raw(); SBAR();
      wo_offsets(); SBAR();
      const float dt = bflo(pk2(expf(bflo(vdt)), 0.f));
      float a2[2], rv[2]; f32x2 cc[2];
#pragma unroll
      for (int bt = 0; bt < 2; ++bt) {
        const float ar = bt ? bfhi(vAr) : bflo(vAr), ai = bt ? bfhi(vAi) : bflo(vAi), br = dt * (bt ? bfhi(vBr) : bflo(vBr)), bi = dt * (bt ? bfhi(vBi) : bflo(vBi));
        const float cr = bt ? bfhi(vCr) : bflo(vCr), ci = bt ? bfhi(vCi) : bflo(vCi);
        a2[bt] = dt * ar * LOG2E; rv[bt] = dt * ai * 0.15915494309189535f;
        cc[bt] = f32x2{cr * br - ci * bi, -(cr * bi + ci * br)};
      }
      amax = wave_max(fmaxf(a2[0], a2[1]));
      const float cmax = wave_max(fmaxf(fmaxf(fabsf(cc[0][0]), fabsf(cc[0][1])), fmaxf(fabsf(cc[1][0]), fabsf(cc[1][1]))));
      ce = __builtin_amdgcn_frexp_expf(cmax);
      const lptr tw = lds + L_TAB + hw * TAB_W + lane * 8, tr_ = lds + L_TAB + hw * TAB_W + r16 * 544 + 16 * g;
      struct C2 { f32x2 a, b; }; struct M2 { CM a, b; };
      auto mul = [](const C2& x, const M2& m) { return C2{cmul(x.a, m.a), cmul(x.b, m.b)}; };
      auto sqr = [](const M2& m) { return M2{mk(cmul(m.a.e, m.a)), mk(cmul(m.b.e, m.b))}; };
      auto TW = [&](int row, const C2& v) { *(LAS u32x2*)(tw + row * 544) = u32x2{pkh(v.a), pkh(v.b)}; };
      auto RD = [&](f16x8 (&f)[8]) {
#pragma unroll
        for (int ks = 0; ks < 8; ++ks) f[ks] = *(const LAS f16x8*)(tr_ + 64 * ks);
      };
      STAMP_AT(1);
      const float an0 = a2[0] - amax, an1 = a2[1] - amax;
      const M2 E1 = {mk(cexp2(an0, -rv[0])), mk(cexp2(an1, -rv[1]))}, E256 = {mk(cexp2(256.f * an0, -256.f * rv[0])), mk(cexp2(256.f * an1, -256.f * rv[1]))};
      const C2 x0 = {f32x2{__builtin_amdgcn_ldexpf(cc[0][0], 14 - ce), __builtin_amdgcn_ldexpf(cc[0][1], 14 - ce)}, f32x2{__builtin_amdgcn_ldexpf(cc[1][0], 14 - ce), __builtin_amdgcn_ldexpf(cc[1][1], 14 - ce)}};
      f16x8 af0[8], af1[8], bfr[8];
      {
        const M2 E2 = sqr(E1), E4 = sqr(E2), E512 = sqr(E256);
        const C2 x1 = mul(x0, E1), x2 = mul(x0, E2), y0 = mul(x0, E256), z0 = mul(x0, E512), x3 = mul(x1, E2);
        TW(0, x0); TW(1, x1); TW(2, x2); TW(3, x3);
        const C2 z1 = mul(y0, E512), y1 = mul(y0, E2);
        TW(4, mul(x0, E4)); TW(5, mul(x1, E4)); TW(6, mul(x2, E4)); TW(7, mul(x3, E4));
        TW(8, y0); TW(9, y1); TW(10, mul(y0, E4)); TW(11, mul(y1, E4));
        TW(12, z0); TW(13, mul(z0, E4)); TW(14, z1); TW(15, mul(z1, E4));
        RD(af0);
        const C2 z2 = mul(z0, E512), z3 = mul(z1, E512), z4 = mul(z2, E512);
        TW(0, z2); TW(1, z3); TW(2, z4); TW(3, mul(z3, E512)); TW(4, mul(z4, E512));
        RD(af1);
      }
      STAMP_AT(6);
      {
        const M2 E8 = {mk(cexp2(8.f * an0, 8.f * rv[0])), mk(cexp2(8.f * an1, 8.f * rv[1]))}, E16 = sqr(E8);
        C2 y[2] = {C2{f32x2{16384.f, 0.f}, f32x2{16384.f, 0.f}}, C2{E8.a.e * 16384.f, E8.b.e * 16384.f}};
#pragma unroll
        for (int yt = 0; yt < 2; ++yt) {
#pragma unroll
          for (int i = 0; i < 8; ++i)
#pragma unroll
            for (int k = 0; k < 2; ++k) { TW(2 * i + k, y[k]); if (yt == 0 || i < 7) y[k] = mul(y[k], E16); }
          RD(bfr);
#pragma unroll
          for (int ks = 0; ks < 8; ++ks) { kacc[0][yt] = MFMA16H(af0[ks], bfr[ks], kacc[0][yt]); kacc[1][yt] = MFMA16H(af1[ks], bfr[ks], kacc[1][yt]); }
          if (yt == 0) STAMP_AT(7);
        }
      }
      STAMP_AT(14);
      uf = __hip_atomic_load((GAS unsigned*)(ws + WS_UF) + (d >> 6) * 32 + c, RLX_AGENT);
      SBAR();
    } else if (kvp) {
      ARGS();
      LOAD_EP();
      const int t = xcd * 8 + (j - 24), tq = hw & 1, sel = hw >> 1;
      {
        const char* wsrc = (sel ? A_->wv : A_->wk) + tq * 2304;
        unsigned o3[3];
#pragma unroll
        for (int i = 0; i < 3; ++i) { const int cc = 64 * i + lane, row = cc / 3, e = cc - 3 * row; o3[i] = rsw(row) * 36 + (e == 2 ? 20 : 16 * e); }
#pragma unroll
        for (int kb = 0; kb < 5; ++kb)
          if (kb < 4 || tq == 0) {
#pragma unroll
            for (int i = 0; i < 3; ++i) glds16(wsrc + kb * 4608, o3[i], lds0 + L_KW + sel * KW_A + tq * 3072 + kb * 6144 + i * 1024);
          }
      }
      STAMP_AT(6);
      SBAR(); PF_DONE(); bar_raw(); SBAR();
      asm volatile("s_waitcnt vmcnt(0)" ::: "memory");
      lds_st(misc + M_HS + hw * 4, 1u); lds_wait<1>(misc + M_HS + (lane & 7) * 4, 1);
      STAMP_AT(8);
      f32x4 aM = {0.f, 0.f, 0.f, 0.f}, aR = aM;
      kv_half(lds, sel, tq, 0, aM, aR);
      lds_wait<0>(misc + M_HS + 16 + hw * 4, 2);
      aM += *(const LAS f32x4*)(lds + hw * 2048 + lane * 16); aR += *(const LAS f32x4*)(lds + hw * 2048 + 1024 + lane * 16);
      STAMP_AT(10);
      float n2 = 0.f;
      const int kh_ = t & 1, ti = t >> 1;
      GET_TAG(tg);
      auto put = [&](int rec, int o, unsigned lo, unsigned hi) {
        __hip_atomic_store((GAS u64*)(ws + WS_KV + rec + o), (u64)lo | ((u64)hi << 32), RLX_AGENT);
      };
      if (sel == 0) {
        const int rk = (ti + 31) & 31, rec = (rk >> 2) * KV_CHUNK + ((rk & 3) * 2 + kh_) * KV_REC + KV_V;
        const unsigned k01 = pk2(aM[0], aM[1]), k23 = pk2(aM[2], aM[3]), kr = pk2(aR[2], aR[3]);
        put(rec, (g >> 1) * 512 + (16 * tq + r16) * 16 + (g & 1) * 8, k01, k23);
        if (g == 0) put(rec, 1024 + (16 * tq + r16) * 8, kr, 0x3f80u);
        n2 = sq2(k01) + sq2(k23) + (g == 0 ? sq2(kr) : 0.f);
      } else {
        const int rec = (ti >> 2) * KV_CHUNK + ((ti & 3) * 2 + kh_) * KV_REC, vo = tq * 608 + (g & 1) * 304 + (g >> 1) * 8;
        put(rec, vo + r16 * 16, pk2(aM[0], aM[1]), pk2(aM[2], aM[3]));
        if (r16 >= 2 && r16 < 5) put(rec, vo + (14 + r16) * 16, r16 == 4 ? 0x3f803f80u : pk2(aR[0], aR[1]), r16 == 4 ? 0x3f803f80u : pk2(aR[2], aR[3]));
      }
      cfence(); lds_st(misc + M_HS + hw * 4, 2u);
      n2 = row_max(sum_rows(n2));
      if (lane == 0) __hip_atomic_fetch_max((LAS unsigned*)(misc + M_N2), __float_as_uint(n2), __ATOMIC_RELAXED, __HIP_MEMORY_SCOPE_WORKGROUP);
      asm volatile("s_waitcnt vmcnt(0)" ::: "memory");
      if (lane == 0) {
        if (__hip_atomic_fetch_add((LAS unsigned*)(misc + M_CNT), 1u, __ATOMIC_RELAXED, __HIP_MEMORY_SCOPE_WORKGROUP) == 3u)
        { __hip_atomic_store((GAS u64*)(ws + WS_KVF + t * 128), ((u64)tg << 32) | lds_ld(misc + M_N2), RLX_AGENT); if (HOLD == 2) asm volatile("s_waitcnt vmcnt(0)" ::: "memory"); cfence(); lds_st(misc + M_CNT, 5u); }
      }
      STAMP_AT(1); PROBE_AT(5);
      wo_offsets();
    } else {
      ep = tu_task(xcd * 24 + j);
      STAMP_AT(1);
      wo_offsets();
    }
    ARGS();
    GET_TAG(tag);
    if (kvp) {
      const u64 t0 = __builtin_amdgcn_s_memrealtime();
      if (hw == 3) {
        u64 fv;
        do fv = __hip_atomic_load((GAS u64*)(ws + WS_KVF + lane * 128), RLX_AGENT); while (!__all((unsigned)(fv >> 32) == tag) && !spin_expired(t0));
        __builtin_amdgcn_fence(__ATOMIC_ACQUIRE, "wavefront");
        const float km = wave_max(__uint_as_float((unsigned)fv));
        if (lane == 0) *(LAS u32x2*)(misc + M_TAG) = u32x2{tag, __float_as_uint(km)};
        cfence(); lds_st(misc + M_GO, 1u);
      } else lds_wait<1>(misc + M_GO, 1);
      const int il = hl, kc = il < 160 ? il : 159;
      u32x4 c0[10];
      const u32x4 k0 = __builtin_amdgcn_raw_buffer_load_b128(wsr, WS_KV + 7 * KV_CHUNK + (6 + (kc >= 80)) * KV_REC + KV_V + (kc >= 80 ? kc - 80 : kc) * 16, 0, 16);
#pragma unroll
      for (int i = 0; i < 10; ++i) c0[i] = __builtin_amdgcn_raw_buffer_load_b128(wsr, WS_KV + il * 16 + i * 4096, 0, 16);
      SBAR();
      SBAR();
      *(LAS u32x4*)(lds + L_K0 + kc * 16) = k0;
#pragma unroll
      for (int i = 0; i < 5; ++i) *(LAS u32x4*)(lds + L_KV + il * 16 + i * 4096) = c0[i];
      cfence(); lds_st(misc + M_R0 + hw * 4, 1u);
      lds_wait<1>(misc + M_QF + p4 * 4, 6);
#pragma unroll
      for (int i = 5; i < 10; ++i) *(LAS u32x4*)(lds + L_KV + il * 16 + i * 4096) = c0[i];
      cfence(); lds_st(misc + M_RDY + hw * 4, 4u);
      STAMP_AT(3);
    }
    u32x4 cv[3];
    const lptr rdyw = misc + M_RDY + hw * 4, donep = misc + M_DONE + (lane & 7) * 4;
    int nh = 4;
    unsigned need = 1;
    auto service_dn = [&](unsigned dn) {
      if (__builtin_expect(__all(dn >= need), 0)) {
        cfence();
        if (need > 1) {
          const lptr dst = lds + L_KV + ((nh >> 1) & 1) * KV_CHUNK + (nh & 1) * 9984 + hl * 16;
          *(LAS u32x4*)dst = cv[0]; *(LAS u32x4*)(dst + 4096) = cv[1];
          if (hl < 112) *(LAS u32x4*)(dst + 8192) = cv[2];
          ++nh;
          cfence(); lds_st(rdyw, nh < 16 ? nh : 99);
        }
        const int o = (nh >> 1) * KV_CHUNK + (nh & 1) * 9984;
#pragma unroll
        for (int i = 0; i < 3; ++i) cv[i] = __builtin_amdgcn_raw_buffer_load_b128(wsr, WS_KV + hl * 16 + i * 4096, o, 16);
        need = nh < 16 ? nh - 2 : 0xffffffffu;
      }
    };
    auto service = [&]() { service_dn(lds_ld(donep)); };
    const lptr kc_ = lds + L_KC + hw * KC_CH;
    auto wcopy = [&](int it, const unsigned (&e)[6]) {
      unsigned al[5];
#pragma unroll
      for (int i = 0; i < 5; ++i) al[i] = __builtin_amdgcn_alignbit(e[i + 1], e[i], 16);
      const lptr dst = kc_ + (lane + 64 * it) * 16;
      *(LAS u32x4*)(dst) = u32x4{e[0], e[1], e[2], e[3]};
      *(LAS u32x4*)(dst + KC_COPY) = u32x4{al[0], al[1], al[2], al[3]};
      *(LAS u32x4*)(dst + 2 * KC_COPY) = u32x4{e[1], e[2], e[3], e[4]};
      *(LAS u32x4*)(dst + 3 * KC_COPY) = u32x4{al[1], al[2], al[3], al[4]};
    };
    auto poll = [&](const char* p) {
      const u64 t0 = __builtin_amdgcn_s_memrealtime();
      while (!__all(__hip_atomic_load((GAS unsigned*)p, RLX_AGENT) == tag) && !spin_expired(t0)) {
        service();
        __builtin_amdgcn_s_sleep(8);
      }
      __builtin_amdgcn_fence(__ATOMIC_ACQUIRE, "wavefront");
    };
    if (own) {
      {
        const float ec = (float)(ce - 28);
        const lptr kq = lds + L_TAB + hw * TAB_W;
        const float b0 = g < 2 ? (float)(4 * g) : g == 2 ? 256.f : 512.f, d1 = g < 2 ? 1.f : g == 2 ? 2.f : 4.f, d2 = g < 2 ? 2.f : g == 2 ? 4.f : 256.f;
#pragma unroll
        for (int yt = 0; yt < 2; ++yt) {
          const int cc_ = 16 * yt + r16;
          const float c8 = (float)(8 * cc_), l0 = c8 + b0, l1 = c8 + (float)(1024 + 1024 * g);
          float v[4], z[4];
#pragma unroll
          for (int e = 0; e < 4; ++e) {
            v[e] = kacc[0][yt][e] * EX(__builtin_fmaf(amax, l0 + (e & 1 ? d1 : 0.f) + (e & 2 ? d2 : 0.f), ec));
            z[e] = kacc[1][yt][e] * EX(__builtin_fmaf(amax, l1 + 256.f * e, ec));
          }
          const int i0 = g < 2 ? 8 * cc_ + 4 * g : g == 2 ? 256 + 4 * cc_ : 384 + 2 * cc_;
          *(LAS unsigned*)(kq + i0 * 2) = pk2(v[0], v[1]);
          *(LAS unsigned*)(kq + (i0 + (g < 3 ? 2 : 64)) * 2) = pk2(v[2], v[3]);
          const unsigned z01 = pk2(z[0], z[1]), z23 = pk2(z[2], z[3]);
          if (g == 0) {
            *(LAS u16*)(kq + (512 + cc_) * 2) = (u16)z01; *(LAS u16*)(kq + (544 + cc_) * 2) = (u16)(z01 >> 16);
            *(LAS u16*)(kq + (576 + cc_) * 2) = (u16)z23; *(LAS u16*)(kq + (608 + cc_) * 2) = (u16)(z23 >> 16);
          } else if (lane == 16 && yt == 0) *(LAS u16*)(kq + 640 * 2) = (u16)z01;
        }
      }
      STAMP_AT(2);
      if (__all(uf == tag)) {
        __builtin_amdgcn_fence(__ATOMIC_ACQUIRE, "wavefront");
#pragma unroll
        for (int i = 0; i < 4; ++i) ut[i] = __builtin_amdgcn_raw_buffer_load_b128(wsr, WS_UT + d * 4096 + lane * 16 + i * 1024, 0, 16);
        have_u = true;
      }
      SBAR();
      unsigned dd[4][6];
      {
        const lptr kq = lds + L_TAB + hw * TAB_W, sc = kq + 2048;
#pragma unroll
        for (int it = 0; it < 2; ++it) {
          const int M = 255 - 64 * it - lane;
          const unsigned lo = *(const LAS u16*)(kq + (384 + M) * 2), hi = *(const LAS u16*)(kq + (385 + M) * 2);
          dd[it][0] = hi | (hi << 16); dd[it][2] = dd[it][3] = lo | (lo << 16);
          dd[it][1] = (M & 1) ? dd[it][0] : hi | (lo << 16);
        }
        {
          const int n = 256 + 2 * (127 - lane);
          const unsigned p = *(const LAS unsigned*)(kq + n * 2), t = *(const LAS u16*)(kq + (n + 2) * 2);
          dd[2][0] = t | (t << 16); dd[2][1] = __builtin_amdgcn_perm(p, p, 0x03020302u); dd[2][2] = __builtin_amdgcn_perm(p, p, 0x01000302u); dd[2][3] = __builtin_amdgcn_perm(p, p, 0x01000100u);
        }
        {
          const int M = 63 - lane;
          const lptr pp = kq + (h ? 8 * M : 128 + 4 * M) * 2;
          const u32x2 p = *(const LAS u32x2*)pp, q = *(const LAS u32x2*)(pp + 8);
          dd[3][0] = h ? __builtin_amdgcn_perm(q[1], q[1], 0x01000302u) : __builtin_amdgcn_perm(p[1], q[0], 0x07060100u);
          dd[3][1] = h ? __builtin_amdgcn_perm(q[0], q[0], 0x01000302u) : __builtin_amdgcn_perm(p[1], p[1], 0x01000100u);
          dd[3][2] = h ? __builtin_amdgcn_perm(p[1], p[1], 0x01000302u) : __builtin_amdgcn_perm(p[0], p[1], 0x07060100u);
          dd[3][3] = h ? __builtin_amdgcn_perm(p[0], p[0], 0x01000302u) : __builtin_amdgcn_perm(p[0], p[0], 0x01000100u);
        }
#pragma unroll
        for (int it = 0; it < 4; ++it) *(LAS u32x2*)(sc + (lane + 64 * it) * 8) = u32x2{dd[it][0], dd[it][1]};
        if (lane == 0) *(LAS u32x2*)(sc + 2048) = u32x2{0u, 0u};
#pragma unroll
        for (int it = 0; it < 4; ++it) { const u32x2 n = *(const LAS u32x2*)(sc + (lane + 64 * it) * 8 + 8); dd[it][4] = n[0]; dd[it][5] = n[1]; }
      }
      if (!have_u) {
        poll(ws + WS_UF + ((d >> 6) * 32 + c) * 4);
#pragma unroll
        for (int i = 0; i < 4; ++i) ut[i] = __builtin_amdgcn_raw_buffer_load_b128(wsr, WS_UT + d * 4096 + lane * 16 + i * 1024, 0, 16);
      }
      if (lane < 32) *(LAS u32x4*)(lds + L_TAB + hw * TAB_W + lane * 16) = u32x4{0u, 0u, 0u, 0u};
#pragma unroll
      for (int i = 0; i < 4; ++i) *(LAS u32x4*)(lds + L_TAB + hw * TAB_W + 512 + i * 1024 + lane * 16) = ut[i];
      STAMP_AT(3);
      lds_wait<2>(misc + M_QF + p4 * 4, 9);
      STAMP_AT(4);
#pragma unroll
      for (int it = 3; it >= 0; --it) wcopy(it, dd[it]);
      if (lane < 16) *(LAS u32x4*)(kc_ + (lane >> 2) * KC_COPY + 4096 + (lane & 3) * 16) = u32x4{0u, 0u, 0u, 0u};
      cfence(); lds_st(misc + M_HS + hw * 4, 1u);
    }
    if (ssm) {
#pragma unroll 1
      for (int o_ = own ? hw : 0; o_ < (EXP == 13 ? 0 : 3); o_ += own ? 3 : 1) {
        const lptr pp = (o_ < 2 ? lds + L_KC + 3 * KC_CH + o_ * 8192 : lds + L_TAB + 3 * TAB_W) + lane * 16;
        if (!own) { while (!__all(lds_ld(misc + M_HS + o_ * 4) != 0u)) { service(); __builtin_amdgcn_s_sleep(4); } cfence(); }
        STAMP_AT(5);
        f32x4 y[8];
        lptr ka = lds + L_KC + o_ * KC_CH + (3 - p4) * KC_COPY + (2028 - 32 * 56 - 4 * q4 + 8 * g) * 2 - (own ? 0 : RSPLIT * 64);
        lptr ua = lds + L_TAB + o_ * TAB_W + 512 - 32 + 32 * r16 + 16 * g - (own ? 0 : RSPLIT * 64);
        bf16x8 af[8], bfr[8];
        auto lda = [&](int i) { af[i] = cat8(*(const volatile LAS s16x4*)(ka + (7 - i) * 512), *(const volatile LAS s16x4*)(ka + (7 - i) * 512 + 8)); };
        auto ldb = [&](int i) { bfr[i] = lds_b128(ua + i * 512); };
#pragma unroll
        for (int i = 0; i < 8; ++i) { ldb(7 - i); lda(i); }
        if (own) {
          const int rel = 16 + r16 - 8 * g;
          u32x4 df;
#pragma unroll
          for (int e = 0; e < 4; ++e) df[e] = (rel >> 1) == e ? dsk << (16 * (rel & 1)) : 0u;
#pragma unroll
          for (int i = 7; i >= 0; --i) y[i] = MFMA16(PB(df), bfr[i], (f32x4{0.f, 0.f, 0.f, 0.f}));
        } else {
#pragma unroll
          for (int i = 0; i < 8; ++i) y[i] = f32x4{0.f, 0.f, 0.f, 0.f};
        }
#pragma unroll 1
        for (int r = own ? RSPLIT : 8 - RSPLIT; r > 0; --r) {
          ka -= 64; ua -= 64;
          PIN(ka); PIN(ua);
          unsigned dn = lds_ld(donep);
          SBAR();
#pragma unroll
          for (int a_ = 0; a_ < 8; ++a_) {
#pragma unroll
            for (int i = 7; i >= a_; --i) { y[i] = MFMA16(af[a_], bfr[i - a_], y[i]); SBAR(); if (CN >= 0) asm volatile("s_nop %0" :: "i"(CN)); SBAR(); }
            lda(a_); ldb(7 - a_); SBAR();
            if (a_ == 0) service_dn(dn);
            if (a_ == 2) dn = lds_ld(donep);
            if (a_ == 3) service_dn(dn);
            SBAR();
          }
        }
        STAMP_AT(9);
        if (!own) {
#pragma unroll
          for (int i = 0; i < 8; ++i) *(LAS f32x4*)(pp + i * 1024) = y[i];
          cfence(); lds_st(misc + M_HS + 16 + o_ * 4, 1u);
        } else {
          while (!__all(lds_ld(misc + M_HS + 16 + hw * 4) != 0u)) { service(); __builtin_amdgcn_s_sleep(4); }
          cfence();
          const lptr ys = lds + L_TAB + hw * TAB_W;
#pragma unroll
          for (int i = 0; i < 8; ++i) { const f32x4 v = y[i] + *(const LAS f32x4*)(pp + i * 1024); *(LAS u32x2*)(ys + i * 512 + r16 * 32 + g * 8) = u32x2{pk2(v[0], v[1]), pk2(v[2], v[3])}; }
          cfence(); lds_st(misc + M_HS + hw * 4, 2u);
#pragma unroll
          for (int i = 0; i < 4; ++i) __builtin_amdgcn_raw_buffer_store_b128(lds_u128(ys + i * 1024 + lane * 16), wsr, WS_YT + d * 4096 + lane * 16 + i * 1024, 0, 16);
          asm volatile("s_waitcnt vmcnt(0)" ::: "memory");
          if (lane == 0) __hip_atomic_store((GAS unsigned*)(ws + WS_YF) + d, tag, RLX_AGENT);
          service();
          STAMP_AT(10);
        }
      }
    }
    lds_wait<4>(donep, 1);
    if (ssm && !own && EXP != 13) { while (!__all(lds_ld(misc + M_HS + (lane & 1) * 4) == 2u)) { service(); __builtin_amdgcn_s_sleep(SPIN_SLEEP); } cfence(); }
#pragma unroll 1
    for (int hq = 2 * hw; hq < 2 * hw + 2; ++hq) {
      const char* src = A_->wo + hg * 144 + (size_t)hq * 72 * ROWB;
#pragma unroll
      for (int i = 0; i < 10; ++i) glds16(src, woff[i], lds0 + L_WO + hq * 10368 + i * 1024);
      glds16_8lanes(src, woff[10], lds0 + L_WO + hq * 10368 + 10240);
      service();
    }
    STAMP_AT(11);
    if (kvp && EXP != 13) {
      while (nh < TYN) { service(); __builtin_amdgcn_s_sleep(SPIN_SLEEP); }
#pragma unroll 1
      for (int task = (xcd * 8 + (j - 24)) * 4 + hw; task < 288; task += 256) {
        poll(ws + WS_YF + ((task >> 5) * 64 + lane) * 4);
        u32x4 ra[8], o[8];
#pragma unroll
        for (int e = 0; e < 8; ++e) ra[e] = __builtin_amdgcn_raw_buffer_load_b128(wsr, WS_YT + ((task >> 5) * 64 + (lane & 7) * 8 + e) * 4096 + (task & 31) * 128 + (lane >> 3) * 16, 0, 16);
        tr8(ra, o);
        const unsigned dst = ((task & 31) * 64 + (lane >> 3) * 8) * (2 * ROWB) + (task >> 5) * 128 + (lane & 7) * 16;
#pragma unroll
        for (int e = 0; e < 8; ++e) __builtin_amdgcn_raw_buffer_store_b128(o[e], OUTR(), dst + e * 2 * ROWB, 0, YAUX);
        service();
      }
      STAMP_AT(9);
    }
    while (nh < 16) { service(); __builtin_amdgcn_s_sleep(SPIN_SLEEP); }
    asm volatile("s_waitcnt vmcnt(0) lgkmcnt(0)" ::: "memory");
    lds_st(misc + M_WOR + hw * 4, 1u);
    STAMP_AT(12);
    STAMP_AT(13);
    lds_wait(misc + M_WOR + p4 * 4, 1);
  }
  ARGS();
  const unsigned tag = *(const LAS unsigned*)(misc + M_TAG);
  const int rt = w & 3, kt = w >> 2;
  bf16x8 wf[6][5];
  const lptr wwa = lds + L_WO + (192 * kt + 8 * g + 2 * q4) * 144 + 8 * p4;
  auto wread = [&](int ks) {
#pragma unroll
    for (int t = 0; t < 5; ++t) wf[ks][t] = cat8(lds_tr(wwa + ks * 32 * 144 + 32 * t), lds_tr(wwa + ks * 32 * 144 + 32 * t + 144));
  };
#pragma unroll
  for (int ks = 0; ks < 5; ++ks) wread(ks);
  if (w < 8) STAMP_AT(5);
  if (w >= 8) lds_wait<8>(misc + M_OXR + (lane & 7) * 4, 1);
  if (w == 11) {
    const u64 t0 = __builtin_amdgcn_s_memrealtime();
    unsigned m;
    do {
      m = (unsigned)__builtin_amdgcn_ballot_w64(__hip_atomic_load((GAS unsigned*)(ws + WS_OF) + (rb * 8 + (lane & 7)) * 16, RLX_AGENT) == tag) & 0xffu;
      if (__builtin_amdgcn_s_memrealtime() - t0 > 400000ull) m = 0xffu;
      lds_st(misc + M_GO2, m);
      if (PSL) __builtin_amdgcn_s_sleep(PSL);
    } while (m != 0xffu);
  } else {
    const unsigned nd = kt == 0 ? 0x07u : kt == 1 ? 0x3cu : 0xe0u;
    while (!__all((lds_ld(misc + M_GO2) & nd) == nd)) __builtin_amdgcn_s_sleep(1);
  }
  __builtin_amdgcn_fence(__ATOMIC_ACQUIRE, "wavefront"); cfence();
  if (w < 8) STAMP_AT(6);
  f32x4 acc[5] = {};
  {
    u32x4 of[6];
#pragma unroll
    for (int ks = 0; ks < 6; ++ks) of[ks] = __builtin_amdgcn_raw_buffer_load_b128(wsr, WS_O2 + (((rb * 4 + rt) * 18 + 6 * kt + ks) * 64 + lane) * 16, 0, 16);
#pragma unroll
    for (int ks = 0; ks < 6; ++ks) {
#pragma unroll
      for (int t = 0; t < 5; ++t) acc[t] = MFMA16(wf[ks][t], PB(of[ks]), acc[t]);
      if (ks == 0) { SBAR(); wread(5); SBAR(); }
    }
  }
  if (w < 8) STAMP_AT(9);
  lds_wait<1>(misc + M_OCNT, 8);
#pragma unroll
  for (int t = 0; t < 5; ++t) if (t < 4 || g < 2) *(LAS f32x4*)(lds + (kt == 2 ? L_OX : L_PART + kt * 19456) + (rt * 16 + r16) * 304 + t * 64 + g * 16) = acc[t];
  bar();
  if (w < 8) STAMP_AT(10);
  if (w < 9) {
    const int tok = tid / 9, cell = tid - 9 * tok;
    const lptr pp = lds + tok * 304 + cell * 32;
    const f32x4 s0 = *(const LAS f32x4*)(pp + L_PART) + *(const LAS f32x4*)(pp + L_PART + 19456) + *(const LAS f32x4*)(pp + L_OX);
    const f32x4 s1 = *(const LAS f32x4*)(pp + L_PART + 16) + *(const LAS f32x4*)(pp + L_PART + 19456 + 16) + *(const LAS f32x4*)(pp + L_OX + 16);
    __builtin_amdgcn_raw_buffer_store_b128(u32x4{pk2(s0[0], s0[1]), pk2(s0[2], s0[3]), pk2(s1[0], s1[1]), pk2(s1[2], s1[3])}, OUTR(), (rb * 64 + tok) * (2 * ROWB) + ROWB + hg * 144 + cell * 16, 0, OAUX);
  } else if (b == 0 && w == 11) {
    const u64 t0 = __builtin_amdgcn_s_memrealtime();
    for (;;) {
      bool ok = true;
#pragma unroll
      for (int k = 0; k < 4; ++k) ok &= __hip_atomic_load((GAS unsigned*)(ws + WS_OF) + (lane + 64 * k) * 16, RLX_AGENT) == tag;
      if (__all(ok) || spin_expired(t0)) break;
    }
    if (lane == 0) __hip_atomic_store((GAS unsigned*)(ws + WS_EPOCH), tag, RLX_AGENT);
  }
  STAMP_FLUSH(0);
}

extern "C" void kernel_launch(void* const* d_in, const int* in_sizes, int n_in, void* d_out, int out_size, void* d_ws, size_t ws_size, hipStream_t stream) {
  Args a;
  a.u = (const char*)d_in[0]; a.Are = (const char*)d_in[1]; a.Aim = (const char*)d_in[2]; a.Bre = (const char*)d_in[3]; a.Bim = (const char*)d_in[4];
  a.Cre = (const char*)d_in[5]; a.Cim = (const char*)d_in[6]; a.ldt = (const char*)d_in[7]; a.Dsk = (const char*)d_in[8]; a.x = (const char*)d_in[9];
  a.wq = (const char*)d_in[10]; a.wk = (const char*)d_in[11]; a.wv = (const char*)d_in[12]; a.wo = (const char*)d_in[13];
  a.out = (char*)d_out; a.ws = (char*)d_ws;
  mega_kernel<<<256, 768, 0, stream>>>(a);
}
```

```cpp
#include <hip/hip_runtime.h>
#include <hip/hip_bf16.h>

typedef unsigned short u16;
typedef unsigned long long u64;
typedef __attribute__((ext_vector_type(8))) __bf16 bf16x8;
typedef __attribute__((ext_vector_type(2))) __bf16 bf16x2;
typedef __attribute__((ext_vector_type(8))) _Float16 f16x8;
typedef __attribute__((ext_vector_type(2))) _Float16 f16x2;
typedef __attribute__((ext_vector_type(4))) short s16x4;
typedef __attribute__((ext_vector_type(8))) short s16x8;
typedef __attribute__((ext_vector_type(4))) float f32x4;
typedef __attribute__((ext_vector_type(2))) float f32x2;
typedef __attribute__((ext_vector_type(16))) float f32x16;
typedef __attribute__((ext_vector_type(4))) unsigned u32x4;
typedef __attribute__((ext_vector_type(2))) unsigned u32x2;
#define LAS __attribute__((address_space(3)))
#define GAS __attribute__((address_space(1)))
typedef LAS char* lptr;
#define DI __device__ __forceinline__
#define SBAR() __builtin_amdgcn_sched_barrier(0)
#define PIN(x) asm volatile("" : "+v"(x))
#define RLX_AGENT __ATOMIC_RELAXED, __HIP_MEMORY_SCOPE_AGENT

constexpr int ROWB = 1152;
constexpr int KV_V = 1216, KV_REC = 2496, KV_CHUNK = 20480;
constexpr unsigned WS_EPOCH = 0, WS_KVF = 256, WS_UF = 8704, WS_OF = 16384, WS_YF = 32768, WS_KV = 40960, WS_O2 = WS_KV + 8 * KV_CHUNK;
constexpr unsigned WS_UT = WS_O2 + 2048 * ROWB, WS_YT = WS_UT + 2048 * ROWB, WS_END = WS_YT + 2048 * ROWB;
constexpr int L_KV = 0, L_KC = 40960, KC_COPY = 4160, KC_CH = 20736, L_QX = L_KC + 4 * KC_COPY, L_K0 = 123904;
constexpr int L_TAB = 128512, TAB_W = 8704, L_WO = L_KC, L_OX = 132608, L_PART = 0, L_MISC = 163328;
constexpr int M_TAG = 0, M_KMAX = 4, M_GO = 8, M_GO2 = 12, M_CNT = 16, M_N2 = 20, M_OCNT = 24, M_WOR = 48, M_DONE = 64, M_RDY = 96, M_OXR = 224, M_QL = 128, M_QF = 144, M_HS = 160, M_R0 = 192;
constexpr int QS = 17408, L_QR = 23552, L_KW = L_QR + 2 * QS, KW_A = 27648, L_KX = L_KW + 2 * KW_A;
constexpr int CODE_LINES = 296;
constexpr int RSPLIT = 6;
constexpr u64 SPIN_LIMIT = 20000000ull;
constexpr unsigned EP_STEP = 0x9E3779B9u;
constexpr float LOG2E = 1.4426950408889634f;
constexpr float QSCALE = 0.23570226039551584f * LOG2E;


DI unsigned pk2(float lo, float hi) { f32x2 v = {lo, hi}; bf16x2 b = __builtin_convertvector(v, bf16x2); return __builtin_bit_cast(unsigned, b); }
DI unsigned pkh(f32x2 v) { f16x2 b = __builtin_convertvector(v, f16x2); return __builtin_bit_cast(unsigned, b); }
DI float bflo(unsigned v) { return __uint_as_float(v << 16); }
DI float bfhi(unsigned v) { return __uint_as_float(v & 0xffff0000u); }
DI float sq2(unsigned v) { return bflo(v) * bflo(v) + bfhi(v) * bfhi(v); }

DI void glds16(const void* sbase, unsigned voff, unsigned lds_dst) {
  unsigned sv;
  asm volatile("s_mov_b32 %0, m0\n\ts_mov_b32 m0, %3\n\ts_nop 0\n\tglobal_load_lds_dwordx4 %1, %2\n\ts_mov_b32 m0, %0"
               : "=&s"(sv) : "v"(voff), "s"(sbase), "s"(lds_dst) : "memory");
}
DI void glds16_8lanes(const void* sbase, unsigned voff, unsigned lds_dst) {
  unsigned sv;
  asm volatile("s_mov_b32 %0, m0\n\ts_mov_b32 m0, %3\n\ts_mov_b64 exec, 0xff\n\tglobal_load_lds_dwordx4 %1, %2\n\ts_mov_b64 exec, -1\n\ts_mov_b32 m0, %0"
               : "=&s"(sv) : "v"(voff), "s"(sbase), "s"(lds_dst) : "memory");
}
DI void bar() { asm volatile("s_waitcnt lgkmcnt(0)\n\ts_barrier" ::: "memory"); }
DI void bar_raw() { asm volatile("s_barrier" ::: "memory"); }
DI bf16x8 lds_b128(lptr p) { return *(const LAS bf16x8*)p; }
DI u32x4 lds_u128(lptr p) { return *(const LAS u32x4*)p; }
DI s16x4 lds_tr(lptr p) { return __builtin_amdgcn_ds_read_tr16_b64_v4i16((LAS s16x4*)p); }
DI bf16x8 cat8(s16x4 a, s16x4 b) { s16x8 v = __builtin_shufflevector(a, b, 0, 1, 2, 3, 4, 5, 6, 7); return __builtin_bit_cast(bf16x8, v); }
#define MFMA16(a, b, c) __builtin_amdgcn_mfma_f32_16x16x32_bf16(a, b, c, 0, 0, 0)
#define MFMA32(a, b, c) __builtin_amdgcn_mfma_f32_32x32x16_bf16(a, b, c, 0, 0, 0)
#define MFMA16H(a, b, c) __builtin_amdgcn_mfma_f32_16x16x32_f16(a, b, c, 0, 0, 0)
#define MFMA32K8(a, b, c) __builtin_amdgcn_mfma_f32_32x32x8bf16_1k(a, b, c, 0, 0, 0)
#define EX(v) __builtin_amdgcn_exp2f(v)
#define PB(P) __builtin_bit_cast(bf16x8, P)

template <int CTRL> DI float dpp_mov(float v) { return __uint_as_float(__builtin_amdgcn_update_dpp(0u, __float_as_uint(v), CTRL, 0xf, 0xf, true)); }
DI float row_max(float v) {
  v = fmaxf(v, dpp_mov<0xB1>(v)); v = fmaxf(v, dpp_mov<0x4E>(v)); v = fmaxf(v, dpp_mov<0x141>(v)); v = fmaxf(v, dpp_mov<0x140>(v));
  return v;
}
DI float wave_max(float v) {
  v = row_max(v);
  auto r1 = __builtin_amdgcn_permlane16_swap(__float_as_uint(v), __float_as_uint(v), false, false);
  v = fmaxf(__uint_as_float(r1[0]), __uint_as_float(r1[1]));
  auto r2 = __builtin_amdgcn_permlane32_swap(__float_as_uint(v), __float_as_uint(v), false, false);
  return fmaxf(__uint_as_float(r2[0]), __uint_as_float(r2[1]));
}
DI float sum_rows(float v) {
  auto r1 = __builtin_amdgcn_permlane16_swap(__float_as_uint(v), __float_as_uint(v), false, false);
  v = __uint_as_float(r1[0]) + __uint_as_float(r1[1]);
  auto r2 = __builtin_amdgcn_permlane32_swap(__float_as_uint(v), __float_as_uint(v), false, false);
  return __uint_as_float(r2[0]) + __uint_as_float(r2[1]);
}
DI float other_half(float v) { auto rr = __builtin_amdgcn_permlane32_swap(__float_as_uint(v), __float_as_uint(v), false, false); return (threadIdx.x & 32) ? __uint_as_float(rr[0]) : __uint_as_float(rr[1]); }
DI bool spin_expired(u64 t0) { __builtin_amdgcn_s_sleep(4); return __builtin_amdgcn_s_memrealtime() - t0 > SPIN_LIMIT; }
struct CM { f32x2 e, r; };
DI CM mk(f32x2 e) { return CM{e, f32x2{-e[1], e[0]}}; }
DI f32x2 cmul(f32x2 a, const CM& m) { return __builtin_elementwise_fma(f32x2{a[1], a[1]}, m.r, f32x2{a[0], a[0]} * m.e); }
DI f32x2 cexp2(float lg, float rev) { rev -= __builtin_rintf(rev); const float e = EX(lg); return f32x2{e * __builtin_amdgcn_cosf(rev), e * __builtin_amdgcn_sinf(rev)}; }

DI unsigned lds_ld(lptr p) { return *(const volatile LAS unsigned*)p; }
DI void lds_st(lptr p, unsigned v) { *(volatile LAS unsigned*)p = v; }
DI void cfence() { asm volatile("" ::: "memory"); }
template <int SL = 8> DI void lds_wait(lptr p, unsigned need) { while (!__all(lds_ld(p) >= need)) __builtin_amdgcn_s_sleep(SL); cfence(); }
DI int rsw(int r) { return (r & ~7) | ((r & 1) << 2) | ((r >> 1) & 3); }
DI void tr8(const u32x4 (&r)[8], u32x4 (&o)[8]) {
#pragma unroll
  for (int k = 0; k < 8; ++k)
#pragma unroll
    for (int m = 0; m < 4; ++m) o[k][m] = __builtin_amdgcn_perm(r[2 * m + 1][k >> 1], r[2 * m][k >> 1], (k & 1) ? 0x07060302u : 0x05040100u);
}

DI void kv_half(lptr lds, int sel, int tq, int k0, f32x4& aM, f32x4& aR) {
  const int lane = threadIdx.x & 63, r16 = lane & 15, g = lane >> 4, q4 = (lane & 15) >> 2, p4 = lane & 3;
  const lptr ma_ = lds + L_KW + sel * KW_A + (8 * g + 2 * q4) * 48 + k0 * 1536, ra_ = ma_ + 40, mb_ = ma_ + 8 * p4;
  lptr xa[2];
#pragma unroll
  for (int ks = 0; ks < 2; ++ks) xa[ks] = lds + L_KX + ((k0 + ks) >> 1) * 4096 + (16 * tq + r16) * 128 + (((4 * ((k0 + ks) & 1) + g) ^ ((r16 >> 1) & 7)) * 16);
  bf16x8 xf[3], fm[3], fr[3];
  auto rd = [&](int i) { xf[i % 3] = lds_b128(xa[i & 1] + (i >> 1) * 4096); fm[i % 3] = cat8(lds_tr(mb_ + i * 1536), lds_tr(mb_ + i * 1536 + 48)); fr[i % 3] = cat8(lds_tr(ra_ + i * 1536), lds_tr(ra_ + i * 1536 + 48)); };
  rd(0); rd(1);
  if (sel == 0) {
#pragma unroll
    for (int i = 0; i < 9; ++i) { if (i + 2 < 9) rd(i + 2); SBAR(); aM = MFMA16(fm[i % 3], xf[i % 3], aM); aR = MFMA16(fr[i % 3], xf[i % 3], aR); SBAR(); }
  } else {
#pragma unroll
    for (int i = 0; i < 9; ++i) { if (i + 2 < 9) rd(i + 2); SBAR(); aM = MFMA16(xf[i % 3], fm[i % 3], aM); aR = MFMA16(xf[i % 3], fr[i % 3], aR); SBAR(); }
  }
}

struct Args { const char *u, *Are, *Aim, *Bre, *Bim, *Cre, *Cim, *ldt, *Dsk, *x, *wq, *wk, *wv, *wo; char *out, *ws; };
typedef const __attribute__((address_space(4))) Args* ArgP;
#define ARGS() ArgP A_ = (ArgP)__builtin_amdgcn_kernarg_segment_ptr(); asm volatile("" : "+s"(A_)); char* const ws = A_->ws; \
  const __amdgpu_buffer_rsrc_t wsr = __builtin_amdgcn_make_buffer_rsrc(ws, 0, WS_END, 0x00020000)
#define OUTR() __builtin_amdgcn_make_buffer_rsrc(A_->out, 0, 2048 * 2 * ROWB, 0x00020000)

__global__ __launch_bounds__(768, 3) void mega_kernel(Args) {
  __shared__ __attribute__((aligned(16))) char smem[163840];
  const lptr lds = (lptr)smem, misc = lds + L_MISC;
  const unsigned lds0 = (unsigned)(size_t)lds;
  const int tid = threadIdx.x, lane = tid & 63, w = __builtin_amdgcn_readfirstlane(tid >> 6);
  const int b = blockIdx.x, xcd = b & 7, j = b >> 3, rb = xcd * 4 + (j & 3), hg = j >> 2;
  const int c = lane & 31, h = lane >> 5, r16 = lane & 15, g = lane >> 4, q4 = (lane & 15) >> 2, p4 = lane & 3;
  const f32x16 zero = {};
  const bool kvp = j >= 24, ssm = !kvp;
  unsigned pf0, pf1;
  asm volatile("s_load_dword %0, %2, 0x0\n\ts_load_dword %1, %2, 0x40" : "=&s"(pf0), "=&s"(pf1) : "s"(__builtin_amdgcn_kernarg_segment_ptr()));
  unsigned pfc = 0;
  if (w < 4) {
    const int ln = (j * 4 + w) * 3 + (lane < 2 ? lane : 2);
    u64 pc;
    asm volatile("s_getpc_b64 %1\n\tglobal_load_dword %0, %2, %1" : "=&v"(pfc), "=&s"(pc) : "v"((ln < CODE_LINES ? ln : 0) * 128));
  }
#define PF_DONE() asm volatile("s_waitcnt lgkmcnt(0)" :: "s"(pf0), "s"(pf1))

  auto tu_task = [&](int task) {
    ARGS();
    const unsigned e0 = __hip_atomic_load((GAS unsigned*)(ws + WS_EPOCH), RLX_AGENT);
    const char* src = A_->u + (size_t)((task & 31) * 64 + (lane >> 3) * 8) * ROWB + (task >> 5) * 128 + (lane & 7) * 16;
    u32x4 ra[8], o[8];
#pragma unroll
    for (int e = 0; e < 8; ++e) ra[e] = *(const u32x4*)(src + e * ROWB);
    SBAR(); PF_DONE(); bar_raw(); SBAR();
    tr8(ra, o);
    if (w == 11) { PIN(o[0]); PIN(o[7]); cfence(); lds_st(misc + M_CNT, 5u); }
#pragma unroll
    for (int e = 0; e < 8; ++e) __builtin_amdgcn_raw_buffer_store_b128(o[e], wsr, WS_UT + ((task >> 5) * 64 + (lane & 7) * 8 + e) * 4096 + (task & 31) * 128 + (lane >> 3) * 16, 0, 16);
    asm volatile("s_waitcnt vmcnt(0)" ::: "memory");
    if (lane == 0) __hip_atomic_store((GAS unsigned*)(ws + WS_UF) + task, e0 + EP_STEP, RLX_AGENT);
    return e0;
  };

  if (w < 8) {
    const int hd = w & 3, kh = w >> 2;
    if (ssm) __builtin_amdgcn_s_setprio(3);
    if (w == 0) lds_st(misc + lane * 4, 0u);

    u32x4 qau, qbu; u32x2 qa, qb; unsigned q2lo, nm; bool fast, prepped = false;
    auto qprep = [&]() {
      qau = lds_u128(lds + L_QX + hd * KC_CH + (32 * kh + c) * 48 + 16 * h);
      qbu = lds_u128(lds + L_QX + hd * KC_CH + (32 * (kh ^ 1) + c) * 48 + 16 * h);
      q2lo = *(const LAS unsigned*)(lds + L_QX + hd * KC_CH + ((lane + 32 * kh) & 63) * 48 + 32);
      const float pa = sq2(qau[0]) + sq2(qau[1]) + sq2(qau[2]) + sq2(qau[3]), pb = sq2(qbu[0]) + sq2(qbu[1]) + sq2(qbu[2]) + sq2(qbu[3]);
      auto rr = __builtin_amdgcn_permlane32_swap(__float_as_uint(pa), __float_as_uint(pb), false, false);
      float n2 = __uint_as_float(rr[0]) + __uint_as_float(rr[1]) + sq2(q2lo);
      PIN(n2);
      lds_wait<1>(misc + M_GO, 1);
      const float bound = __builtin_amdgcn_sqrtf(n2 * *(const LAS float*)(misc + M_KMAX)) * 1.01f;
      fast = __all(bound <= 40.f);
      nm = fast ? pk2(-bound, 0.f) : 0u;
      qa = h ? u32x2{0u, 0u} : u32x2{q2lo, nm}; qb = h ? u32x2{q2lo, nm} : u32x2{0u, 0u};
    };
    if (w >= 4) {
      ARGS();
      unsigned ep7 = 0;
      if (w == 7) ep7 = __hip_atomic_load((GAS unsigned*)(ws + WS_EPOCH), RLX_AGENT);
      const int iw = w - 4, rot = (2 * (hg + (j & 3))) % 9;
      unsigned xo[2], wo_[3];
#pragma unroll
      for (int i = 0; i < 2; ++i) { const int xrow = 16 * iw + 8 * i + (lane >> 3); xo[i] = xrow * ROWB + (((lane & 7) ^ ((xrow >> 1) & 7)) * 16); }
#pragma unroll
      for (int i = 0; i < 3; ++i) { const int cc = (i < 2 ? 128 * iw + 64 * i : 512) + lane; wo_[i] = rsw(cc / 9) * ROWB + (cc % 9) * 16; }
      const char* xb = A_->x + (size_t)rb * 64 * ROWB;
      const char* wb = A_->wq + hg * 144;
      const lptr qlw = misc + M_QL + iw * 4;
      PF_DONE(); bar();
      int st = rot, slot = 0;
#pragma unroll 1
      for (int s = 0; s < 9; ++s) {
        if (s == 0 && ssm) lds_wait<2>(misc + M_CNT, 5);
        if (s == 2 && kvp) {
          asm volatile("s_waitcnt vmcnt(0)" ::: "memory"); lds_st(qlw, 2u);
          lds_wait<2>(misc + M_CNT, 5);
        }
        if (s >= 5) lds_wait<1>(misc + M_QF + p4 * 4, s - 4);
        const unsigned dst = lds0 + L_QR + slot * QS;
        const char* xs = xb + st * 128; const char* wsrc = wb + (size_t)st * 64 * ROWB;
        glds16(xs, xo[0], dst + iw * 2048); glds16(xs, xo[1], dst + iw * 2048 + 1024);
        glds16(wsrc, wo_[0], dst + 8192 + iw * 2048); glds16(wsrc, wo_[1], dst + 8192 + iw * 2048 + 1024);
        if (iw == 0) { glds16(wsrc, wo_[2], dst + 16384); asm volatile("s_waitcnt vmcnt(10)" ::: "memory"); }
        else asm volatile("s_waitcnt vmcnt(8)" ::: "memory");
        if (ssm || s > 2) lds_st(qlw, s > 0 ? s - 1 : 0);
        st = st == 8 ? 0 : st + 1; slot = slot == 4 ? 0 : slot + 1;
      }
      if (w == 4) asm volatile("s_waitcnt vmcnt(5)" ::: "memory"); else asm volatile("s_waitcnt vmcnt(4)" ::: "memory");
      lds_st(qlw, 8u);
      unsigned tag;
      asm volatile("s_waitcnt vmcnt(0)\n\tv_add_u32 %0, %2, %1" : "=v"(tag) : "v"(ep7), "s"(EP_STEP) : "memory");
      lds_st(qlw, 9u);
      const u64 t0 = __builtin_amdgcn_s_memrealtime();
      if (w == 7) {
        u64 fv;
        do fv = __hip_atomic_load((GAS u64*)(ws + WS_KVF + lane * 128), RLX_AGENT); while (!__all((unsigned)(fv >> 32) == tag) && !spin_expired(t0));
        __builtin_amdgcn_fence(__ATOMIC_ACQUIRE, "wavefront");
        const float km = wave_max(__uint_as_float((unsigned)fv));
        if (lane == 0) *(LAS u32x2*)(misc + M_TAG) = u32x2{tag, __float_as_uint(km)};
        cfence(); lds_st(misc + M_GO, 1u);
      } else lds_wait<1>(misc + M_GO, 1);
      const int il = tid - 256, kc = il < 160 ? il : 159;
      u32x4 c0[10];
      const u32x4 k0 = __builtin_amdgcn_raw_buffer_load_b128(wsr, WS_KV + 7 * KV_CHUNK + (6 + (kc >= 80)) * KV_REC + KV_V + (kc >= 80 ? kc - 80 : kc) * 16, 0, 16);
#pragma unroll
      for (int i = 0; i < 10; ++i) c0[i] = __builtin_amdgcn_raw_buffer_load_b128(wsr, WS_KV + il * 16 + i * 4096, 0, 16);
      SBAR();
      if (__all(lds_ld(misc + M_QF + p4 * 4) >= 10u)) { cfence(); qprep(); prepped = true; }
      SBAR();
      *(LAS u32x4*)(lds + L_K0 + kc * 16) = k0;
#pragma unroll
      for (int i = 0; i < 5; ++i) *(LAS u32x4*)(lds + L_KV + il * 16 + i * 4096) = c0[i];
      cfence(); lds_st(misc + M_R0 + iw * 4, 1u);
      if (!prepped) lds_wait<1>(misc + M_QF + p4 * 4, 6);
#pragma unroll
      for (int i = 5; i < 10; ++i) *(LAS u32x4*)(lds + L_KV + il * 16 + i * 4096) = c0[i];
      cfence(); lds_st(misc + M_RDY + iw * 4, 4u);
    } else {
      lptr xa[2];
#pragma unroll
      for (int ks = 0; ks < 2; ++ks) xa[ks] = lds + L_QR + (16 * w + r16) * 128 + (((4 * ks + g) ^ ((r16 >> 1) & 7)) * 16);
      const lptr wa = lds + L_QR + 8192 + (8 * g + 2 * q4) * 144 + 8 * p4;
      const lptr qlr = misc + M_QL + p4 * 4, qfw = misc + M_QF + w * 4;
      f32x4 acc[5] = {};
      if (kvp) {
        ARGS();
        const int xrow = 8 * w + (lane >> 3);
        const unsigned xo = xrow * ROWB + (((lane & 7) ^ ((xrow >> 1) & 7)) * 16);
        const char* xb = A_->x + (size_t)(xcd * 8 + (j - 24)) * 32 * ROWB;
#pragma unroll
        for (int i = 0; i < 9; ++i) glds16(xb + i * 128, xo, lds0 + L_KX + i * 4096 + w * 1024);
      }
      if (j < 12 && w == 3) tu_task(192 + xcd * 12 + j);
      else { PF_DONE(); bar(); }
      if (kvp) {
        asm volatile("s_waitcnt vmcnt(0)" ::: "memory"); lds_st(misc + M_HS + 16 + w * 4, 1u);
        lds_wait<1>(misc + M_HS + (lane & 7) * 4, 1);
        f32x4 aM = {0.f, 0.f, 0.f, 0.f}, aR = aM;
        kv_half(lds, w >> 1, w & 1, 9, aM, aR);
        *(LAS f32x4*)(lds + w * 2048 + lane * 16) = aM; *(LAS f32x4*)(lds + w * 2048 + 1024 + lane * 16) = aR;
        cfence(); lds_st(misc + M_HS + 16 + w * 4, 2u);
      }
      lptr qw[10];
#pragma unroll
      for (int i = 0; i < 10; ++i) { const int n = 16 * (i >> 1) + 4 * g + 2 * (i & 1), hh = n / 18; qw[i] = n < 72 ? lds + L_QX + hh * KC_CH + (16 * w + r16) * 48 + (n - 18 * hh) * 2 : lds + L_K0 + 2560 + lane * 4; PIN(qw[i]); }
      bf16x8 xf[2][2], wf[2][2][5];
      asm volatile("s_waitcnt vmcnt(0)" :: "v"(pfc));
      lds_wait<1>(qlr, 1);
#pragma unroll
      for (int s = 0; s < 10; ++s) {
        const int so = (s % 5) * QS, bf = s & 1;
        unsigned ql = 0;
#pragma unroll
        for (int i = 0; i < 10; ++i) {
          const int ks = i / 5, t = i % 5;
          if (s < 9) {
            if (i == 0 && s < 8) ql = lds_ld(qlr);
            if (t == 0) xf[bf][ks] = lds_b128(xa[ks] + so);
            wf[bf][ks][t] = cat8(lds_tr(wa + so + ks * 32 * 144 + 32 * t), lds_tr(wa + so + ks * 32 * 144 + 32 * t + 144));
            if (i == 9) { cfence(); lds_st(qfw, s + 1); }
          }
          if (s > 0) { acc[t] = MFMA16(wf[bf ^ 1][ks][t], xf[bf ^ 1][ks], acc[t]); PIN(acc[t]); }
          SBAR();
        }
        if (s < 8 && !__all(ql >= (unsigned)(s + 2))) lds_wait<1>(qlr, s + 2);
        cfence(); SBAR();
      }
      lds_wait<0>(misc + M_QF + p4 * 4, 9);
#pragma unroll
      for (int t = 0; t < 5; ++t)
#pragma unroll
        for (int e = 0; e < 2; ++e) {
          *(LAS unsigned*)qw[2 * t + e] = pk2(acc[t][2 * e] * QSCALE, acc[t][2 * e + 1] * QSCALE);
        }
      cfence(); lds_st(qfw, 10u);
    }
    if (!prepped) { lds_wait<1>(misc + M_QF + p4 * 4, 10); qprep(); }
    const bf16x8 q1a = PB(qau), q1b = PB(qbu);
    lptr vb = lds + L_KV + kh * KV_REC + h * 304 + (c < 18 ? c * 16 : (c == 18 || c == 22) ? 18 * 16 : 0x40000);
    lptr kb = lds + L_KV + kh * KV_REC + KV_V + lane * 16;
    lptr k2b = lds + L_KV + kh * KV_REC + KV_V + 1024 + c * 8;
    const lptr syncr = misc + M_DONE + r16 * 4, donep = misc + M_DONE + w * 4;
    auto ring_ok = [&](unsigned v, unsigned need) { return ((unsigned)__builtin_amdgcn_ballot_w64(v >= need) & 0xf00u) == 0xf00u; };
    auto ring_wait = [&](unsigned need) { while (!ring_ok(lds_ld(syncr), need)) __builtin_amdgcn_s_sleep(2); cfence(); };
    f32x16 Oa = zero, Ob = zero;
    PIN(qa); PIN(qb); PIN(Oa); PIN(Ob);
    const s16x4 q2a = __builtin_bit_cast(s16x4, qa), q2b = __builtin_bit_cast(s16x4, qb);
    lds_wait<1>(misc + M_R0 + p4 * 4, 1);
    __builtin_amdgcn_s_setprio(1);
    s16x4 kf2 = *(const LAS s16x4*)(lds + L_K0 + kh * 1280 + 1024 + c * 8);
    bf16x8 kf = lds_b128(lds + L_K0 + kh * 1280 + lane * 16);
    cfence(); lds_st(donep, 1u);
    float ma = 0.f, mb = 0.f;
    if (fast) {
      f32x16 Aa, Ab, Ba, Bb;
      Aa = MFMA32K8(kf2, q2a, zero); Aa = MFMA32(kf, q1a, Aa);
      Ab = MFMA32K8(kf2, q2b, zero); Ab = MFMA32(kf, q1b, Ab);
      SBAR(); kf2 = *(const LAS s16x4*)k2b; SBAR(); bf16x8 vf0 = lds_b128(vb); SBAR(); kf = lds_b128(kb); SBAR(); bf16x8 vf1 = lds_b128(vb + 608); SBAR();
      u32x4 p0, p1;
#define GRP(MF, RD, S, i, P, jj) do { float e0 = EX(S[i]), e1 = EX(S[i + 1]); SBAR(); MF; RD; SBAR(); float e2 = EX(S[i + 2]), e3 = EX(S[i + 3]); P[jj] = pk2(e0, e1); P[jj + 1] = pk2(e2, e3); PIN(P); SBAR(); } while (0)
#define HALF1(Ca, Cb, Na, Nb, RD) do { \
      GRP(Na = MFMA32K8(kf2, q2a, zero), , Ca, 4, p0, 2); \
      GRP(Oa = MFMA32(vf0, PB(p0), Oa), RD, Ca, 8, p1, 0); \
      GRP(Na = MFMA32(kf, q1a, Na), , Ca, 12, p1, 2); \
      GRP(Oa = MFMA32(vf1, PB(p1), Oa), , Cb, 0, p0, 0); } while (0)
#define HALF2(Cb, Na, Nb, off) do { \
      GRP(Nb = MFMA32K8(kf2, q2b, zero), kf2 = *(const LAS s16x4*)(k2b + (off)), Cb, 4, p0, 2); \
      GRP(Ob = MFMA32(vf0, PB(p0), Ob), vf0 = lds_b128(vb + (off)), Cb, 8, p1, 0); \
      GRP(Nb = MFMA32(kf, q1b, Nb), kf = lds_b128(kb + (off)), Cb, 12, p1, 2); \
      GRP(Ob = MFMA32(vf1, PB(p1), Ob), vf1 = lds_b128(vb + (off) + 608), Na, 0, p0, 0); } while (0)
      p0[0] = pk2(EX(Aa[0]), EX(Aa[1])); p0[1] = pk2(EX(Aa[2]), EX(Aa[3])); p0[2] = 0; p0[3] = 0; p1 = p0;
#define RWAIT(val, need) do { if (__builtin_expect(!ring_ok(val, need), 0)) ring_wait(need); } while (0)
#define BALANCE(val, mine) asm volatile("s_cmp_lt_u32 %0, %1\n\ts_setprio 1\n\ts_cbranch_scc0 1f\n\ts_setprio 0\n1:" :: "s"((unsigned)__builtin_amdgcn_readlane(val, w ^ 4)), "s"(mine) : "scc")
      for (unsigned p = 0; p < 8; ++p) {
        unsigned sa;
        HALF1(Aa, Ab, Ba, Bb, ); HALF2(Ab, Ba, Bb, 2 * KV_REC);
        cfence(); lds_st(donep, 2 * p + 2); SBAR();
        HALF1(Ba, Bb, Aa, Ab, sa = lds_ld(syncr));
        BALANCE(sa, 2 * p + 2);
        RWAIT(sa, 2 * p + 2);
        cfence(); SBAR();
        HALF2(Bb, Aa, Ab, 4 * KV_REC);
        HALF1(Aa, Ab, Ba, Bb, ); HALF2(Ab, Ba, Bb, 6 * KV_REC);
        cfence(); lds_st(donep, 2 * p + 3); SBAR();
        HALF1(Ba, Bb, Aa, Ab, sa = lds_ld(syncr));
        BALANCE(sa, 2 * p + 3);
        const int adv = (p & 1) ? -KV_CHUNK : KV_CHUNK;
        vb += adv; kb += adv; k2b += adv;
        RWAIT(sa, 2 * p + 3);
        cfence(); SBAR();
        HALF2(Bb, Aa, Ab, 0);
      }
    } else {
      ma = -INFINITY; mb = -INFINITY;
      for (int i = 0; i < 32; ++i) {
        if (i && (i & 1) == 0) ring_wait((i >> 1) + 1);
        const int off = ((i >> 2) & 1) * KV_CHUNK + (i & 3) * 2 * KV_REC;
        f32x16 Sa = MFMA32K8(kf2, q2a, zero), Sb = MFMA32K8(kf2, q2b, zero);
        Sa = MFMA32(kf, q1a, Sa); Sb = MFMA32(kf, q1b, Sb);
        kf = lds_b128(kb + off); kf2 = *(const LAS s16x4*)(k2b + off);
        const bf16x8 vf0 = lds_b128(vb + off), vf1 = lds_b128(vb + off + 608);
        if (i & 1) { cfence(); lds_st(donep, (i >> 1) + 2); }
        float ta = ma, tb = mb;
#pragma unroll
        for (int r = 0; r < 16; ++r) { ta = fmaxf(ta, Sa[r]); tb = fmaxf(tb, Sb[r]); }
        ta = fmaxf(ta, other_half(ta)); tb = fmaxf(tb, other_half(tb));
        const float fa = EX(ma - ta), fb = EX(mb - tb);
        ma = ta; mb = tb;
        u32x4 pp[4];
#pragma unroll
        for (int r = 0; r < 8; ++r) {
          Oa[2 * r] *= fa; Oa[2 * r + 1] *= fa; Ob[2 * r] *= fb; Ob[2 * r + 1] *= fb;
          pp[r >> 2][r & 3] = pk2(EX(Sa[2 * r] - ma), EX(Sa[2 * r + 1] - ma));
          pp[2 + (r >> 2)][r & 3] = pk2(EX(Sb[2 * r] - mb), EX(Sb[2 * r + 1] - mb));
        }
        Oa = MFMA32(vf0, PB(pp[0]), Oa); Ob = MFMA32(vf0, PB(pp[2]), Ob);
        Oa = MFMA32(vf1, PB(pp[1]), Oa); Ob = MFMA32(vf1, PB(pp[3]), Ob);
      }
    }
    __builtin_amdgcn_s_setprio(0);
    ARGS();
    lds_wait<2>(misc + M_WOR + p4 * 4, 1);
    {
      const lptr ox = lds + L_OX + w * 3072 + lane * 16;
      *(LAS f32x4*)(ox) = f32x4{Ob[0], Ob[1], Ob[2], Ob[3]};
      *(LAS f32x4*)(ox + 1024) = f32x4{Ob[4], Ob[5], Ob[6], Ob[7]};
      *(LAS f32x4*)(ox + 2048) = f32x4{Ob[8], Ob[9], Ob[10], mb};
      cfence(); lds_st(misc + M_OXR + w * 4, 1u);
    }
    lds_wait<2>(misc + M_OXR + (w ^ 4) * 4, 1);
    {
      const lptr ox = lds + L_OX + (w ^ 4) * 3072 + lane * 16;
      const f32x4 t0 = *(const LAS f32x4*)(ox), t1 = *(const LAS f32x4*)(ox + 1024), t2 = *(const LAS f32x4*)(ox + 2048);
      const float mm = fmaxf(ma, t2[3]);
      float fa = EX(ma - mm), fo = EX(t2[3] - mm);
      const float inv = __builtin_amdgcn_rcpf(Oa[10] * fa + t2[2] * fo);
      fa *= inv; fo *= inv;
      const int tok = 32 * kh + c, gm = 9 * hg + 2 * hd + h, g8 = 9 * hg + 8;
      const unsigned ob = WS_O2 + ((rb * 4 + (tok >> 4)) * 72 * 16 + (tok & 15)) * 16;
      const u32x4 v = {pk2(Oa[0] * fa + t0[0] * fo, Oa[1] * fa + t0[1] * fo), pk2(Oa[2] * fa + t0[2] * fo, Oa[3] * fa + t0[3] * fo),
                       pk2(Oa[4] * fa + t1[0] * fo, Oa[5] * fa + t1[1] * fo), pk2(Oa[6] * fa + t1[2] * fo, Oa[7] * fa + t1[3] * fo)};
      __builtin_amdgcn_raw_buffer_store_b128(v, wsr, ob + gm * 256, 0, 16);
      if (h == 0) __builtin_amdgcn_raw_buffer_store_b32(pk2(Oa[8] * fa + t2[0] * fo, Oa[9] * fa + t2[1] * fo), wsr, ob + g8 * 256 + hd * 4, 0, 16);
      const unsigned tg = *(const LAS unsigned*)(misc + M_TAG);
      asm volatile("s_waitcnt vmcnt(0)" ::: "memory");
      if (lane == 0) {
        if (__hip_atomic_fetch_add((LAS unsigned*)(misc + M_OCNT), 1u, __ATOMIC_RELAXED, __HIP_MEMORY_SCOPE_WORKGROUP) == 7u)
          __hip_atomic_store((GAS unsigned*)(ws + WS_OF) + (rb * 8 + hg) * 16, tg, RLX_AGENT);
      }
    }
  } else {
    const int hw = w - 8, hl = tid - 512;
    __builtin_amdgcn_s_setprio(2);
    unsigned ep;
#define LOAD_EP() ep = __hip_atomic_load((GAS unsigned*)(ws + WS_EPOCH), RLX_AGENT)
#define GET_TAG(t) unsigned t; asm volatile("v_add_u32 %0, %2, %1" : "=v"(t) : "v"(ep), "s"(EP_STEP))
    const int d = xcd * 72 + j * 3 + hw;
    const bool own = ssm && hw < 3;
    unsigned woff[11];
    auto wo_offsets = [&]() {
#pragma unroll
      for (int i = 0; i < 11; ++i) {
        const int cc = 64 * i + (i < 10 ? lane : lane & 7), row = cc / 9, kk = rsw(row), cl = kk >> 3, e = kk & 7;
        woff[i] = (cl < 8 ? 18 * (cl >> 1) + 4 * (cl & 1) + (e & 3) + 2 * (e & 4) : 18 * (e >> 1) + 16 + (e & 1)) * ROWB + (cc - 9 * row) * 16;
        PIN(woff[i]);
      }
    };
    unsigned dsk = 0;
    f32x4 kacc[2][2] = {}; float amax = 0.f; int ce = 0;
    u32x4 ut[4]; bool have_u = false; unsigned uf = 0;
    if (own) {
      ARGS();
      const unsigned po = d * 256 + lane * 4;
      const unsigned vdt = *(const u16*)(A_->ldt + d * 2);
      const unsigned vAr = *(const unsigned*)(A_->Are + po), vAi = *(const unsigned*)(A_->Aim + po), vBr = *(const unsigned*)(A_->Bre + po), vBi = *(const unsigned*)(A_->Bim + po);
      const unsigned vCr = *(const unsigned*)(A_->Cre + po), vCi = *(const unsigned*)(A_->Cim + po);
      dsk = *(const u16*)(A_->Dsk + d * 2);
      LOAD_EP();
      SBAR(); PF_DONE(); bar_raw(); SBAR();
      wo_offsets(); SBAR();
      const float dt = bflo(pk2(expf(bflo(vdt)), 0.f));
      float a2[2], rv[2]; f32x2 cc[2];
#pragma unroll
      for (int bt = 0; bt < 2; ++bt) {
        const float ar = bt ? bfhi(vAr) : bflo(vAr), ai = bt ? bfhi(vAi) : bflo(vAi), br = dt * (bt ? bfhi(vBr) : bflo(vBr)), bi = dt * (bt ? bfhi(vBi) : bflo(vBi));
        const float cr = bt ? bfhi(vCr) : bflo(vCr), ci = bt ? bfhi(vCi) : bflo(vCi);
        a2[bt] = dt * ar * LOG2E; rv[bt] = dt * ai * 0.15915494309189535f;
        cc[bt] = f32x2{cr * br - ci * bi, -(cr * bi + ci * br)};
      }
      amax = wave_max(fmaxf(a2[0], a2[1]));
      const float cmax = wave_max(fmaxf(fmaxf(fabsf(cc[0][0]), fabsf(cc[0][1])), fmaxf(fabsf(cc[1][0]), fabsf(cc[1][1]))));
      ce = __builtin_amdgcn_frexp_expf(cmax);
      const lptr tw = lds + L_TAB + hw * TAB_W + lane * 8, tr_ = lds + L_TAB + hw * TAB_W + r16 * 544 + 16 * g;
      struct C2 { f32x2 a, b; }; struct M2 { CM a, b; };
      auto mul = [](const C2& x, const M2& m) { return C2{cmul(x.a, m.a), cmul(x.b, m.b)}; };
      auto sqr = [](const M2& m) { return M2{mk(cmul(m.a.e, m.a)), mk(cmul(m.b.e, m.b))}; };
      auto TW = [&](int row, const C2& v) { *(LAS u32x2*)(tw + row * 544) = u32x2{pkh(v.a), pkh(v.b)}; };
      auto RD = [&](f16x8 (&f)[8]) {
        cfence();
#pragma unroll
        for (int ks = 0; ks < 8; ++ks) f[ks] = *(const LAS f16x8*)(tr_ + 64 * ks);
        cfence();
      };
      const float an0 = a2[0] - amax, an1 = a2[1] - amax;
      const M2 E1 = {mk(cexp2(an0, -rv[0])), mk(cexp2(an1, -rv[1]))}, E256 = {mk(cexp2(256.f * an0, -256.f * rv[0])), mk(cexp2(256.f * an1, -256.f * rv[1]))};
      const C2 x0 = {f32x2{__builtin_amdgcn_ldexpf(cc[0][0], 14 - ce), __builtin_amdgcn_ldexpf(cc[0][1], 14 - ce)}, f32x2{__builtin_amdgcn_ldexpf(cc[1][0], 14 - ce), __builtin_amdgcn_ldexpf(cc[1][1], 14 - ce)}};
      f16x8 af0[8], af1[8], bfr[8];
      {
        const M2 E2 = sqr(E1), E4 = sqr(E2), E512 = sqr(E256);
        const C2 x1 = mul(x0, E1), x2 = mul(x0, E2), y0 = mul(x0, E256), z0 = mul(x0, E512), x3 = mul(x1, E2);
        TW(0, x0); TW(1, x1); TW(2, x2); TW(3, x3);
        const C2 z1 = mul(y0, E512), y1 = mul(y0, E2);
        TW(4, mul(x0, E4)); TW(5, mul(x1, E4)); TW(6, mul(x2, E4)); TW(7, mul(x3, E4));
        TW(8, y0); TW(9, y1); TW(10, mul(y0, E4)); TW(11, mul(y1, E4));
        TW(12, z0); TW(13, mul(z0, E4)); TW(14, z1); TW(15, mul(z1, E4));
        RD(af0);
        const C2 z2 = mul(z0, E512), z3 = mul(z1, E512), z4 = mul(z2, E512);
        TW(0, z2); TW(1, z3); TW(2, z4); TW(3, mul(z3, E512)); TW(4, mul(z4, E512));
        RD(af1);
      }
      {
        const M2 E8 = {mk(cexp2(8.f * an0, 8.f * rv[0])), mk(cexp2(8.f * an1, 8.f * rv[1]))}, E16 = sqr(E8);
        C2 y[2] = {C2{f32x2{16384.f, 0.f}, f32x2{16384.f, 0.f}}, C2{E8.a.e * 16384.f, E8.b.e * 16384.f}};
#pragma unroll
        for (int yt = 0; yt < 2; ++yt) {
#pragma unroll
          for (int i = 0; i < 8; ++i)
#pragma unroll
            for (int k = 0; k < 2; ++k) { TW(2 * i + k, y[k]); if (yt == 0 || i < 7) y[k] = mul(y[k], E16); }
          RD(bfr);
#pragma unroll
          for (int ks = 0; ks < 8; ++ks) { kacc[0][yt] = MFMA16H(af0[ks], bfr[ks], kacc[0][yt]); kacc[1][yt] = MFMA16H(af1[ks], bfr[ks], kacc[1][yt]); }
        }
      }
      uf = __hip_atomic_load((GAS unsigned*)(ws + WS_UF) + (d >> 6) * 32 + c, RLX_AGENT);
      SBAR();
    } else if (kvp) {
      ARGS();
      LOAD_EP();
      const int t = xcd * 8 + (j - 24), tq = hw & 1, sel = hw >> 1;
      {
        const char* wsrc = (sel ? A_->wv : A_->wk) + tq * 2304;
        unsigned o3[3];
#pragma unroll
        for (int i = 0; i < 3; ++i) { const int cc = 64 * i + lane, row = cc / 3, e = cc - 3 * row; o3[i] = rsw(row) * 36 + (e == 2 ? 20 : 16 * e); }
#pragma unroll
        for (int kb = 0; kb < 5; ++kb)
          if (kb < 4 || tq == 0) {
#pragma unroll
            for (int i = 0; i < 3; ++i) glds16(wsrc + kb * 4608, o3[i], lds0 + L_KW + sel * KW_A + tq * 3072 + kb * 6144 + i * 1024);
          }
      }
      SBAR(); PF_DONE(); bar_raw(); SBAR();
      asm volatile("s_waitcnt vmcnt(0)" ::: "memory");
      lds_st(misc + M_HS + hw * 4, 1u); lds_wait<1>(misc + M_HS + (lane & 7) * 4, 1);
      f32x4 aM = {0.f, 0.f, 0.f, 0.f}, aR = aM;
      kv_half(lds, sel, tq, 0, aM, aR);
      lds_wait<0>(misc + M_HS + 16 + hw * 4, 2);
      aM += *(const LAS f32x4*)(lds + hw * 2048 + lane * 16); aR += *(const LAS f32x4*)(lds + hw * 2048 + 1024 + lane * 16);
      float n2 = 0.f;
      const int kh_ = t & 1, ti = t >> 1;
      GET_TAG(tg);
      auto put = [&](int rec, int o, unsigned lo, unsigned hi) {
        __hip_atomic_store((GAS u64*)(ws + WS_KV + rec + o), (u64)lo | ((u64)hi << 32), RLX_AGENT);
      };
      if (sel == 0) {
        const int rk = (ti + 31) & 31, rec = (rk >> 2) * KV_CHUNK + ((rk & 3) * 2 + kh_) * KV_REC + KV_V;
        const unsigned k01 = pk2(aM[0], aM[1]), k23 = pk2(aM[2], aM[3]), kr = pk2(aR[2], aR[3]);
        put(rec, (g >> 1) * 512 + (16 * tq + r16) * 16 + (g & 1) * 8, k01, k23);
        if (g == 0) put(rec, 1024 + (16 * tq + r16) * 8, kr, 0x3f80u);
        n2 = sq2(k01) + sq2(k23) + (g == 0 ? sq2(kr) : 0.f);
      } else {
        const int rec = (ti >> 2) * KV_CHUNK + ((ti & 3) * 2 + kh_) * KV_REC, vo = tq * 608 + (g & 1) * 304 + (g >> 1) * 8;
        put(rec, vo + r16 * 16, pk2(aM[0], aM[1]), pk2(aM[2], aM[3]));
        if (r16 >= 2 && r16 < 5) put(rec, vo + (14 + r16) * 16, r16 == 4 ? 0x3f803f80u : pk2(aR[0], aR[1]), r16 == 4 ? 0x3f803f80u : pk2(aR[2], aR[3]));
      }
      n2 = row_max(sum_rows(n2));
      if (lane == 0) __hip_atomic_fetch_max((LAS unsigned*)(misc + M_N2), __float_as_uint(n2), __ATOMIC_RELAXED, __HIP_MEMORY_SCOPE_WORKGROUP);
      asm volatile("s_waitcnt vmcnt(0)" ::: "memory");
      if (lane == 0) {
        if (__hip_atomic_fetch_add((LAS unsigned*)(misc + M_CNT), 1u, __ATOMIC_RELAXED, __HIP_MEMORY_SCOPE_WORKGROUP) == 3u)
        { __hip_atomic_store((GAS u64*)(ws + WS_KVF + t * 128), ((u64)tg << 32) | lds_ld(misc + M_N2), RLX_AGENT); cfence(); lds_st(misc + M_CNT, 5u); }
      }
      wo_offsets();
    } else {
      ep = tu_task(xcd * 24 + j);
      wo_offsets();
    }
    ARGS();
    GET_TAG(tag);
    u32x4 cv[3];
    const lptr rdyw = misc + M_RDY + hw * 4, donep = misc + M_DONE + (lane & 7) * 4;
    int nh = 4;
    unsigned need = 1;
    auto service_dn = [&](unsigned dn) {
      if (__builtin_expect(__all(dn >= need), 0)) {
        cfence();
        if (need > 1) {
          const lptr dst = lds + L_KV + ((nh >> 1) & 1) * KV_CHUNK + (nh & 1) * 9984 + hl * 16;
          *(LAS u32x4*)dst = cv[0]; *(LAS u32x4*)(dst + 4096) = cv[1];
          if (hl < 112) *(LAS u32x4*)(dst + 8192) = cv[2];
          ++nh;
          cfence(); lds_st(rdyw, nh < 16 ? nh : 99);
        }
        const int o = (nh >> 1) * KV_CHUNK + (nh & 1) * 9984;
#pragma unroll
        for (int i = 0; i < 3; ++i) cv[i] = __builtin_amdgcn_raw_buffer_load_b128(wsr, WS_KV + hl * 16 + i * 4096, o, 16);
        need = nh < 16 ? nh - 2 : 0xffffffffu;
      }
    };
    auto service = [&]() { service_dn(lds_ld(donep)); };
    const lptr kc_ = lds + L_KC + hw * KC_CH;
    auto wcopy = [&](int it, const unsigned (&e)[6]) {
      unsigned al[5];
#pragma unroll
      for (int i = 0; i < 5; ++i) al[i] = __builtin_amdgcn_alignbit(e[i + 1], e[i], 16);
      const lptr dst = kc_ + (lane + 64 * it) * 16;
      *(LAS u32x4*)(dst) = u32x4{e[0], e[1], e[2], e[3]};
      *(LAS u32x4*)(dst + KC_COPY) = u32x4{al[0], al[1], al[2], al[3]};
      *(LAS u32x4*)(dst + 2 * KC_COPY) = u32x4{e[1], e[2], e[3], e[4]};
      *(LAS u32x4*)(dst + 3 * KC_COPY) = u32x4{al[1], al[2], al[3], al[4]};
    };
    auto poll = [&](const char* p) {
      const u64 t0 = __builtin_amdgcn_s_memrealtime();
      while (!__all(__hip_atomic_load((GAS unsigned*)p, RLX_AGENT) == tag) && !spin_expired(t0)) {
        service();
        __builtin_amdgcn_s_sleep(8);
      }
      __builtin_amdgcn_fence(__ATOMIC_ACQUIRE, "wavefront");
    };
    if (own) {
      {
        const float ec = (float)(ce - 28);
        const lptr kq = lds + L_TAB + hw * TAB_W;
        const float b0 = g < 2 ? (float)(4 * g) : g == 2 ? 256.f : 512.f, d1 = g < 2 ? 1.f : g == 2 ? 2.f : 4.f, d2 = g < 2 ? 2.f : g == 2 ? 4.f : 256.f;
#pragma unroll
        for (int yt = 0; yt < 2; ++yt) {
          const int cc_ = 16 * yt + r16;
          const float c8 = (float)(8 * cc_), l0 = c8 + b0, l1 = c8 + (float)(1024 + 1024 * g);
          float v[4], z[4];
#pragma unroll
          for (int e = 0; e < 4; ++e) {
            v[e] = kacc[0][yt][e] * EX(__builtin_fmaf(amax, l0 + (e & 1 ? d1 : 0.f) + (e & 2 ? d2 : 0.f), ec));
            z[e] = kacc[1][yt][e] * EX(__builtin_fmaf(amax, l1 + 256.f * e, ec));
          }
          const int i0 = g < 2 ? 8 * cc_ + 4 * g : g == 2 ? 256 + 4 * cc_ : 384 + 2 * cc_;
          *(LAS unsigned*)(kq + i0 * 2) = pk2(v[0], v[1]);
          *(LAS unsigned*)(kq + (i0 + (g < 3 ? 2 : 64)) * 2) = pk2(v[2], v[3]);
          const unsigned z01 = pk2(z[0], z[1]), z23 = pk2(z[2], z[3]);
          if (g == 0) {
            *(LAS u16*)(kq + (512 + cc_) * 2) = (u16)z01; *(LAS u16*)(kq + (544 + cc_) * 2) = (u16)(z01 >> 16);
            *(LAS u16*)(kq + (576 + cc_) * 2) = (u16)z23; *(LAS u16*)(kq + (608 + cc_) * 2) = (u16)(z23 >> 16);
          } else if (lane == 16 && yt == 0) *(LAS u16*)(kq + 640 * 2) = (u16)z01;
        }
      }
      if (__all(uf == tag)) {
        __builtin_amdgcn_fence(__ATOMIC_ACQUIRE, "wavefront");
#pragma unroll
        for (int i = 0; i < 4; ++i) ut[i] = __builtin_amdgcn_raw_buffer_load_b128(wsr, WS_UT + d * 4096 + lane * 16 + i * 1024, 0, 16);
        have_u = true;
      }
      cfence(); SBAR();
      unsigned dd[4][6];
      {
        const lptr kq = lds + L_TAB + hw * TAB_W, sc = kq + 2048;
#pragma unroll
        for (int it = 0; it < 2; ++it) {
          const int M = 255 - 64 * it - lane;
          const unsigned lo = *(const LAS u16*)(kq + (384 + M) * 2), hi = *(const LAS u16*)(kq + (385 + M) * 2);
          dd[it][0] = hi | (hi << 16); dd[it][2] = dd[it][3] = lo | (lo << 16);
          dd[it][1] = (M & 1) ? dd[it][0] : hi | (lo << 16);
        }
        {
          const int n = 256 + 2 * (127 - lane);
          const unsigned p = *(const LAS unsigned*)(kq + n * 2), t = *(const LAS u16*)(kq + (n + 2) * 2);
          dd[2][0] = t | (t << 16); dd[2][1] = __builtin_amdgcn_perm(p, p, 0x03020302u); dd[2][2] = __builtin_amdgcn_perm(p, p, 0x01000302u); dd[2][3] = __builtin_amdgcn_perm(p, p, 0x01000100u);
        }
        {
          const int M = 63 - lane;
          const lptr pp = kq + (h ? 8 * M : 128 + 4 * M) * 2;
          const u32x2 p = *(const LAS u32x2*)pp, q = *(const LAS u32x2*)(pp + 8);
          dd[3][0] = h ? __builtin_amdgcn_perm(q[1], q[1], 0x01000302u) : __builtin_amdgcn_perm(p[1], q[0], 0x07060100u);
          dd[3][1] = h ? __builtin_amdgcn_perm(q[0], q[0], 0x01000302u) : __builtin_amdgcn_perm(p[1], p[1], 0x01000100u);
          dd[3][2] = h ? __builtin_amdgcn_perm(p[1], p[1], 0x01000302u) : __builtin_amdgcn_perm(p[0], p[1], 0x07060100u);
          dd[3][3] = h ? __builtin_amdgcn_perm(p[0], p[0], 0x01000302u) : __builtin_amdgcn_perm(p[0], p[0], 0x01000100u);
        }
#pragma unroll
        for (int it = 0; it < 4; ++it) *(LAS u32x2*)(sc + (lane + 64 * it) * 8) = u32x2{dd[it][0], dd[it][1]};
        if (lane == 0) *(LAS u32x2*)(sc + 2048) = u32x2{0u, 0u};
        cfence();
#pragma unroll
        for (int it = 0; it < 4; ++it) { const u32x2 n = *(const LAS u32x2*)(sc + (lane + 64 * it) * 8 + 8); dd[it][4] = n[0]; dd[it][5] = n[1]; }
      }
      if (!have_u) {
        poll(ws + WS_UF + ((d >> 6) * 32 + c) * 4);
#pragma unroll
        for (int i = 0; i < 4; ++i) ut[i] = __builtin_amdgcn_raw_buffer_load_b128(wsr, WS_UT + d * 4096 + lane * 16 + i * 1024, 0, 16);
      }
      cfence();
      if (lane < 32) *(LAS u32x4*)(lds + L_TAB + hw * TAB_W + lane * 16) = u32x4{0u, 0u, 0u, 0u};
#pragma unroll
      for (int i = 0; i < 4; ++i) *(LAS u32x4*)(lds + L_TAB + hw * TAB_W + 512 + i * 1024 + lane * 16) = ut[i];
      lds_wait<2>(misc + M_QF + p4 * 4, 9);
#pragma unroll
      for (int it = 3; it >= 0; --it) wcopy(it, dd[it]);
      if (lane < 16) *(LAS u32x4*)(kc_ + (lane >> 2) * KC_COPY + 4096 + (lane & 3) * 16) = u32x4{0u, 0u, 0u, 0u};
      cfence(); lds_st(misc + M_HS + hw * 4, 1u); cfence();
    }
    if (ssm) {
#pragma unroll 1
      for (int o_ = own ? hw : 0; o_ < 3; o_ += own ? 3 : 1) {
        const lptr pp = (o_ < 2 ? lds + L_KC + 3 * KC_CH + o_ * 8192 : lds + L_TAB + 3 * TAB_W) + lane * 16;
        if (!own) { while (!__all(lds_ld(misc + M_HS + o_ * 4) != 0u)) { service(); __builtin_amdgcn_s_sleep(4); } cfence(); }
        f32x4 y[8];
        lptr ka = lds + L_KC + o_ * KC_CH + (3 - p4) * KC_COPY + (2028 - 32 * 56 - 4 * q4 + 8 * g) * 2 - (own ? 0 : RSPLIT * 64);
        lptr ua = lds + L_TAB + o_ * TAB_W + 512 - 32 + 32 * r16 + 16 * g - (own ? 0 : RSPLIT * 64);
        bf16x8 af[8], bfr[8];
        auto lda = [&](int i) { af[i] = cat8(*(const volatile LAS s16x4*)(ka + (7 - i) * 512), *(const volatile LAS s16x4*)(ka + (7 - i) * 512 + 8)); };
        auto ldb = [&](int i) { bfr[i] = lds_b128(ua + i * 512); };
#pragma unroll
        for (int i = 0; i < 8; ++i) { ldb(7 - i); lda(i); }
        if (own) {
          const int rel = 16 + r16 - 8 * g;
          u32x4 df;
#pragma unroll
          for (int e = 0; e < 4; ++e) df[e] = (rel >> 1) == e ? dsk << (16 * (rel & 1)) : 0u;
#pragma unroll
          for (int i = 7; i >= 0; --i) y[i] = MFMA16(PB(df), bfr[i], (f32x4{0.f, 0.f, 0.f, 0.f}));
        } else {
#pragma unroll
          for (int i = 0; i < 8; ++i) y[i] = f32x4{0.f, 0.f, 0.f, 0.f};
        }
#pragma unroll 1
        for (int r = own ? RSPLIT : 8 - RSPLIT; r > 0; --r) {
          ka -= 64; ua -= 64;
          PIN(ka); PIN(ua);
          unsigned dn = lds_ld(donep);
          SBAR();
#pragma unroll
          for (int a_ = 0; a_ < 8; ++a_) {
#pragma unroll
            for (int i = 7; i >= a_; --i) { y[i] = MFMA16(af[a_], bfr[i - a_], y[i]); SBAR(); }
            lda(a_); ldb(7 - a_); SBAR();
            if (a_ == 0) service_dn(dn);
            if (a_ == 2) dn = lds_ld(donep);
            if (a_ == 3) service_dn(dn);
            SBAR();
          }
        }
        if (!own) {
#pragma unroll
          for (int i = 0; i < 8; ++i) *(LAS f32x4*)(pp + i * 1024) = y[i];
          cfence(); lds_st(misc + M_HS + 16 + o_ * 4, 1u);
        } else {
          while (!__all(lds_ld(misc + M_HS + 16 + hw * 4) != 0u)) { service(); __builtin_amdgcn_s_sleep(4); }
          cfence();
          const lptr ys = lds + L_TAB + hw * TAB_W;
#pragma unroll
          for (int i = 0; i < 8; ++i) { const f32x4 v = y[i] + *(const LAS f32x4*)(pp + i * 1024); *(LAS u32x2*)(ys + i * 512 + r16 * 32 + g * 8) = u32x2{pk2(v[0], v[1]), pk2(v[2], v[3])}; }
          cfence(); lds_st(misc + M_HS + hw * 4, 2u); cfence();
#pragma unroll
          for (int i = 0; i < 4; ++i) __builtin_amdgcn_raw_buffer_store_b128(lds_u128(ys + i * 1024 + lane * 16), wsr, WS_YT + d * 4096 + lane * 16 + i * 1024, 0, 16);
          asm volatile("s_waitcnt vmcnt(0)" ::: "memory");
          if (lane == 0) __hip_atomic_store((GAS unsigned*)(ws + WS_YF) + d, tag, RLX_AGENT);
          service();
        }
      }
    }
    lds_wait<4>(donep, 1);
    if (ssm && !own) { while (!__all(lds_ld(misc + M_HS + (lane & 1) * 4) == 2u)) { service(); __builtin_amdgcn_s_sleep(8); } cfence(); }
#pragma unroll 1
    for (int hq = 2 * hw; hq < 2 * hw + 2; ++hq) {
      const char* src = A_->wo + hg * 144 + (size_t)hq * 72 * ROWB;
#pragma unroll
      for (int i = 0; i < 10; ++i) glds16(src, woff[i], lds0 + L_WO + hq * 10368 + i * 1024);
      glds16_8lanes(src, woff[10], lds0 + L_WO + hq * 10368 + 10240);
      service();
    }
    if (kvp) {
      while (nh < 13) { service(); __builtin_amdgcn_s_sleep(8); }
      asm volatile("s_waitcnt vmcnt(0)" ::: "memory");
      lds_st(misc + M_WOR + hw * 4, 1u);
#pragma unroll 1
      for (int task = (xcd * 8 + (j - 24)) * 4 + hw; task < 288; task += 256) {
        poll(ws + WS_YF + ((task >> 5) * 64 + lane) * 4);
        u32x4 ra[8], o[8];
#pragma unroll
        for (int e = 0; e < 8; ++e) ra[e] = __builtin_amdgcn_raw_buffer_load_b128(wsr, WS_YT + ((task >> 5) * 64 + (lane & 7) * 8 + e) * 4096 + (task & 31) * 128 + (lane >> 3) * 16, 0, 16);
        tr8(ra, o);
        const unsigned dst = ((task & 31) * 64 + (lane >> 3) * 8) * (2 * ROWB) + (task >> 5) * 128 + (lane & 7) * 16;
#pragma unroll
        for (int e = 0; e < 8; ++e) __builtin_amdgcn_raw_buffer_store_b128(o[e], OUTR(), dst + e * 2 * ROWB, 0, 16);
        service();
      }
    }
    while (nh < 16) { service(); __builtin_amdgcn_s_sleep(8); }
    asm volatile("s_waitcnt vmcnt(0) lgkmcnt(0)" ::: "memory");
    lds_st(misc + M_WOR + hw * 4, 1u);
    lds_wait(misc + M_WOR + p4 * 4, 1);
  }
  ARGS();
  const unsigned tag = *(const LAS unsigned*)(misc + M_TAG);
  const int rt = w & 3, kt = w >> 2;
  bf16x8 wf[6][5];
  const lptr wwa = lds + L_WO + (192 * kt + 8 * g + 2 * q4) * 144 + 8 * p4;
  auto wread = [&](int ks) {
#pragma unroll
    for (int t = 0; t < 5; ++t) wf[ks][t] = cat8(lds_tr(wwa + ks * 32 * 144 + 32 * t), lds_tr(wwa + ks * 32 * 144 + 32 * t + 144));
  };
#pragma unroll
  for (int ks = 0; ks < 5; ++ks) wread(ks);
  if (w >= 8) lds_wait<8>(misc + M_OXR + (lane & 7) * 4, 1);
  if (w == 11) {
    const u64 t0 = __builtin_amdgcn_s_memrealtime();
    unsigned m;
    do {
      m = (unsigned)__builtin_amdgcn_ballot_w64(__hip_atomic_load((GAS unsigned*)(ws + WS_OF) + (rb * 8 + (lane & 7)) * 16, RLX_AGENT) == tag) & 0xffu;
      if (__builtin_amdgcn_s_memrealtime() - t0 > SPIN_LIMIT) m = 0xffu;
      lds_st(misc + M_GO2, m);
    } while (m != 0xffu);
  } else {
    const unsigned nd = kt == 0 ? 0x07u : kt == 1 ? 0x3cu : 0xe0u;
    while (!__all((lds_ld(misc + M_GO2) & nd) == nd)) __builtin_amdgcn_s_sleep(1);
  }
  __builtin_amdgcn_fence(__ATOMIC_ACQUIRE, "wavefront"); cfence();
  f32x4 acc[5] = {};
  {
    u32x4 of[6];
#pragma unroll
    for (int ks = 0; ks < 6; ++ks) of[ks] = __builtin_amdgcn_raw_buffer_load_b128(wsr, WS_O2 + (((rb * 4 + rt) * 18 + 6 * kt + ks) * 64 + lane) * 16, 0, 16);
#pragma unroll
    for (int ks = 0; ks < 6; ++ks) {
#pragma unroll
      for (int t = 0; t < 5; ++t) acc[t] = MFMA16(wf[ks][t], PB(of[ks]), acc[t]);
      if (ks == 0) { SBAR(); wread(5); SBAR(); }
    }
  }
  lds_wait<1>(misc + M_OCNT, 8);
#pragma unroll
  for (int t = 0; t < 5; ++t) if (t < 4 || g < 2) *(LAS f32x4*)(lds + (kt == 2 ? L_OX : L_PART + kt * 19456) + (rt * 16 + r16) * 304 + t * 64 + g * 16) = acc[t];
  bar();
  if (w < 9) {
    const int tok = tid / 9, cell = tid - 9 * tok;
    const lptr pp = lds + tok * 304 + cell * 32;
    const f32x4 s0 = *(const LAS f32x4*)(pp + L_PART) + *(const LAS f32x4*)(pp + L_PART + 19456) + *(const LAS f32x4*)(pp + L_OX);
    const f32x4 s1 = *(const LAS f32x4*)(pp + L_PART + 16) + *(const LAS f32x4*)(pp + L_PART + 19456 + 16) + *(const LAS f32x4*)(pp + L_OX + 16);
    __builtin_amdgcn_raw_buffer_store_b128(u32x4{pk2(s0[0], s0[1]), pk2(s0[2], s0[3]), pk2(s1[0], s1[1]), pk2(s1[2], s1[3])}, OUTR(), (rb * 64 + tok) * (2 * ROWB) + ROWB + hg * 144 + cell * 16, 0, 0);
  } else if (b == 0 && w == 11) {
    const u64 t0 = __builtin_amdgcn_s_memrealtime();
    for (;;) {
      bool ok = true;
#pragma unroll
      for (int k = 0; k < 4; ++k) ok &= __hip_atomic_load((GAS unsigned*)(ws + WS_OF) + (lane + 64 * k) * 16, RLX_AGENT) == tag;
      if (__all(ok) || spin_expired(t0)) break;
    }
    if (lane == 0) __hip_atomic_store((GAS unsigned*)(ws + WS_EPOCH), tag, RLX_AGENT);
  }
}

extern "C" void kernel_launch(void* const* d_in, const int* in_sizes, int n_in, void* d_out, int out_size, void* d_ws, size_t ws_size, hipStream_t stream) {
  Args a;
  a.u = (const char*)d_in[0]; a.Are = (const char*)d_in[1]; a.Aim = (const char*)d_in[2]; a.Bre = (const char*)d_in[3]; a.Bim = (const char*)d_in[4];
  a.Cre = (const char*)d_in[5]; a.Cim = (const char*)d_in[6]; a.ldt = (const char*)d_in[7]; a.Dsk = (const char*)d_in[8]; a.x = (const char*)d_in[9];
  a.wq = (const char*)d_in[10]; a.wk = (const char*)d_in[11]; a.wv = (const char*)d_in[12]; a.wo = (const char*)d_in[13];
  a.out = (char*)d_out; a.ws = (char*)d_ws;
  mega_kernel<<<256, 768, 0, stream>>>(a);
}
```
